```python
import torch

def _softmax(x, axis=-1):
    return torch.softmax(x, dim=axis)
def _gelu(x):
    return torch.nn.functional.gelu(x, approximate="tanh")
def _silu(x):
    return torch.nn.functional.silu(x)
def _sigmoid(x):
    return torch.sigmoid(x)
def _layernorm(x, axis=-1, eps=1e-5):
    m = x.mean(dim=axis, keepdim=True)
    v = x.var(dim=axis, keepdim=True, unbiased=False)
    return (x - m) / torch.sqrt(v + eps)
def _scatter_add(tgt, idx, src, axis=0):
    return tgt.clone().index_add_(axis, idx, src)
def _segment_sum(src, seg, n):
    out = torch.zeros((n,) + src.shape[1:], dtype=src.dtype)
    return out.index_add_(0, seg, src)
def _pad(x, pw):
    flat = tuple(v for pair in reversed(pw) for v in pair)
    return torch.nn.functional.pad(x, flat)

kernel_name = 'standalone__gqa_block_cached__near_ridge'

def setup_inputs():

    _w = (lambda a: (lambda t: t.to(torch.bfloat16) if t.is_floating_point() else t)(torch.as_tensor(a)))
    B, Sq, D = 8, 144, 448
    Hq, Hkv, Sk, Dh = 16, 8, 1024, 256
    assert Hq % Hkv == 0, (Hq, Hkv)
    return {
        "x": _w((lambda *s: torch.randn(*s, dtype=torch.bfloat16) * 0.8)(B, Sq, D)),
        "k_cache": _w((lambda *s: torch.randn(*s, dtype=torch.bfloat16) * 0.8)(B, Hkv, Sk, Dh)),
        "v_cache": _w((lambda *s: torch.randn(*s, dtype=torch.bfloat16) * 0.05)(B, Hkv, Sk, Dh)),
        "wq": _w((lambda *s: torch.randn(*s, dtype=torch.bfloat16) * 0.8)(D, Hq * Dh) * (D ** -0.5)),
        "wo": _w((lambda *s: torch.randn(*s, dtype=torch.bfloat16) * 0.05)(Hq * Dh, D) * ((Hq * Dh) ** -0.5)),
    }

def reference(x, k_cache, v_cache, wq, wo):
    B, Sq, D = x.shape
    Hq, Dh = 16, 256
    Hkv = k_cache.shape[1]
    rep = Hq // Hkv
    q = (lambda x, *d: x.permute(*d))((x @ wq).reshape(B, Sq, Hq, Dh), 0, 2, 1, 3)
    k = (lambda x, n, axis: torch.repeat_interleave(x, n, dim=axis))(k_cache, rep, axis=1)
    v = (lambda x, n, axis: torch.repeat_interleave(x, n, dim=axis))(v_cache, rep, axis=1)
    scores = torch.einsum("bhqd,bhkd->bhqk", q, k) * (Dh ** -0.5)
    p = _softmax(scores, axis=-1)
    o = torch.einsum("bhqk,bhkd->bhqd", p, v)
    return (lambda x, *d: x.permute(*d))(o, 0, 2, 1, 3).reshape(B, Sq, Hq * Dh) @ wo
```

```cpp
#include <hip/hip_runtime.h>
#include <hip/hip_bf16.h>
typedef __hip_bfloat16 bf16;
constexpr int B=8,SQ=144,D=448,HQ=16,HKV=8,SK=1024,DH=256;
__global__ void qproj(const bf16* x,const bf16* wq,bf16* q){
  int n=blockIdx.x*256+threadIdx.x; int m=blockIdx.y;
  float a=0; for(int k=0;k<D;++k) a+=__bfloat162float(x[m*D+k])*__bfloat162float(wq[(size_t)k*4096+n]);
  q[(size_t)m*4096+n]=__float2bfloat16(a);
}
__global__ void attn(const bf16* q,const bf16* kc,const bf16* vc,bf16* o){
  int m=blockIdx.x,h=blockIdx.y,b=m/SQ,kvh=h/2,t=threadIdx.x;
  __shared__ float qs[DH]; __shared__ float s[SK]; __shared__ float red[256];
  qs[t]=__bfloat162float(q[(size_t)m*4096+h*DH+t]); __syncthreads();
  const bf16* K=kc+((size_t)(b*HKV+kvh))*SK*DH; const bf16* V=vc+((size_t)(b*HKV+kvh))*SK*DH;
  float mx=-1e30f;
  for(int j=t;j<SK;j+=256){ float a=0; for(int d=0;d<DH;++d) a+=qs[d]*__bfloat162float(K[(size_t)j*DH+d]); a*=0.0625f; s[j]=a; mx=fmaxf(mx,a);}
  red[t]=mx; __syncthreads(); for(int o2=128;o2>0;o2>>=1){ if(t<o2) red[t]=fmaxf(red[t],red[t+o2]); __syncthreads(); } mx=red[0]; __syncthreads();
  float sum=0; for(int j=t;j<SK;j+=256){ float e=__expf(s[j]-mx); s[j]=e; sum+=e; }
  red[t]=sum; __syncthreads(); for(int o2=128;o2>0;o2>>=1){ if(t<o2) red[t]+=red[t+o2]; __syncthreads(); } sum=red[0];
  float a=0; for(int j=0;j<SK;++j) a+=s[j]*__bfloat162float(V[(size_t)j*DH+t]);
  o[(size_t)m*4096+h*DH+t]=__float2bfloat16(a/sum);
}
__global__ void oproj(const bf16* o,const bf16* wo,bf16* out){
  int n=threadIdx.x; int m=blockIdx.x; if(n>=D) return;
  float a=0; for(int k=0;k<4096;++k) a+=__bfloat162float(o[(size_t)m*4096+k])*__bfloat162float(wo[(size_t)k*D+n]);
  out[m*D+n]=__float2bfloat16(a);
}
extern "C" void kernel_launch(void* const* d_in, const int* in_sizes, int n_in, void* d_out, int out_size, void* d_ws, size_t ws_size, hipStream_t stream){
  const bf16* x=(const bf16*)d_in[0]; const bf16* kc=(const bf16*)d_in[1]; const bf16* vc=(const bf16*)d_in[2]; const bf16* wq=(const bf16*)d_in[3]; const bf16* wo=(const bf16*)d_in[4];
  bf16* q=(bf16*)d_ws; bf16* o=q+(size_t)B*SQ*4096;
  qproj<<<dim3(16,B*SQ),256,0,stream>>>(x,wq,q);
  attn<<<dim3(B*SQ,HQ),256,0,stream>>>(q,kc,vc,o);
  oproj<<<B*SQ,448,0,stream>>>(o,wo,(bf16*)d_out);
}
```

```cpp
#include <hip/hip_runtime.h>
#include <hip/hip_bf16.h>

typedef __bf16 bf16x8 __attribute__((ext_vector_type(8)));
typedef __bf16 bf16x2 __attribute__((ext_vector_type(2)));
typedef float f32x4 __attribute__((ext_vector_type(4)));
typedef float f32x2 __attribute__((ext_vector_type(2)));
typedef unsigned u32x4 __attribute__((ext_vector_type(4)));
typedef unsigned u32x2 __attribute__((ext_vector_type(2)));

#define DI __device__ __forceinline__

namespace {
constexpr int NB = 8, SQ = 144, DM = 448, HQ = 16, HKV = 8, SK = 1024, DH = 256;
constexpr int ROWS = 72;
constexpr unsigned LDS_K = 0;
constexpr unsigned LDS_P = 131072;
constexpr unsigned LDS_MISC = 151552;
constexpr unsigned LDS_X = 65536;
constexpr unsigned LDS_Q = 110592;
constexpr unsigned LDS_O = 0;
constexpr unsigned LDS_BYTES = 163840;
constexpr unsigned IMG = 10240;
constexpr float CEXP = 0.09016844005556021f;
constexpr float LBIG = 1.8446744e19f;

DI unsigned rfl(unsigned x) { return __builtin_amdgcn_readfirstlane(x); }
DI unsigned cvtpk(float lo, float hi) { f32x2 v = {lo, hi}; bf16x2 b = __builtin_convertvector(v, bf16x2); return __builtin_bit_cast(unsigned, b); }
DI f32x4 mfma(u32x4 a, u32x4 b, f32x4 c) {
  return __builtin_amdgcn_mfma_f32_16x16x32_bf16(__builtin_bit_cast(bf16x8, a), __builtin_bit_cast(bf16x8, b), c, 0, 0, 0);
}
DI void barrier() { asm volatile("s_waitcnt lgkmcnt(0)" ::: "memory"); __builtin_amdgcn_s_barrier(); asm volatile("" ::: "memory"); }
DI void dma16(unsigned lds, unsigned voff, __amdgpu_buffer_rsrc_t srd, unsigned soff) {
  asm volatile("s_mov_b32 m0, %0\n\ts_nop 0\n\tbuffer_load_dwordx4 %1, %2, %3 offen lds" :: "s"(lds), "v"(voff), "s"(srd), "s"(soff) : "memory");
}
struct VSet { u32x2 r[8]; };
DI void vs_issue(VSet& s, __amdgpu_buffer_rsrc_t srd, unsigned voff, unsigned sbase, unsigned stride) {
#pragma unroll
  for (int j = 0; j < 8; ++j) s.r[j] = __builtin_amdgcn_raw_buffer_load_b64(srd, voff, sbase + j * stride, 0);
}
DI void vs_consume(const VSet& s, const u32x4 (&bf)[5], f32x4 (&acc)[4][5]) {
#pragma unroll
  for (int e = 0; e < 4; ++e) {
    u32x4 a;
#pragma unroll
    for (int jj = 0; jj < 4; ++jj)
      a[jj] = __builtin_amdgcn_perm(s.r[2 * jj + 1][e >> 1], s.r[2 * jj][e >> 1], (e & 1) ? 0x07060302u : 0x05040100u);
#pragma unroll
    for (int t = 0; t < 5; ++t) acc[e][t] = mfma(a, bf[t], acc[e][t]);
  }
}
}

__global__ __launch_bounds__(512, 2) void gqa_main(const unsigned short* __restrict__ x, const unsigned short* __restrict__ kc,
                                                   const unsigned short* __restrict__ vc, const unsigned short* __restrict__ wq,
                                                   const unsigned short* __restrict__ wo, unsigned short* __restrict__ part) {
  extern __shared__ __attribute__((aligned(16))) char smem[];
  const unsigned tid = threadIdx.x, lane = tid & 63u, g = lane >> 4, c = lane & 15u;
  const unsigned wv = rfl(tid >> 6), w = wv & 3u;
  const unsigned bid = blockIdx.x, kvh = bid & 7u, jj_ = bid >> 3, b = jj_ >> 2, h = kvh * 2 + ((jj_ >> 1) & 1u), rh = jj_ & 1u;
  const unsigned ldsb = (unsigned)(size_t)smem;
  const unsigned row0 = b * SQ + rh * ROWS;
  const size_t kvoff = (size_t)(b * HKV + kvh) * SK * DH;
  const __amdgpu_buffer_rsrc_t srdK = __builtin_amdgcn_make_buffer_rsrc((void*)(kc + kvoff), (short)0, SK * DH * 2, 0x00020000);
  const __amdgpu_buffer_rsrc_t srdWo = __builtin_amdgcn_make_buffer_rsrc((void*)(wo + (size_t)h * DH * DM), (short)0, DH * DM * 2, 0x00020000);
  const unsigned r8 = lane >> 3, swz = ((lane & 7u) ^ r8) << 4;
  const unsigned voffK = r8 * 512u + swz;
  const unsigned rd0 = c * 128u + ((g ^ (c & 7u)) << 4);
  const unsigned wr0 = c * 128u + (((2u * g) ^ (c & 7u)) << 4);
  float* misc = (float*)(smem + LDS_MISC);

  auto load_b = [&](u32x4 (&bf)[5], unsigned base, int sh) {
#pragma unroll
    for (int t = 0; t < 5; ++t) bf[t] = *(const u32x4*)(smem + base + (rd0 ^ (sh * 64u)) + t * 2048u);
  };
  auto kdma = [&](unsigned blk, unsigned slot) {
#pragma unroll
    for (int p = 0; p < 8; ++p)
      dma16(ldsb + LDS_K + slot * 32768u + w * 8192u + p * 1024u, voffK, srdK, (blk * 64u + w * 16u + (p >> 2) * 8u) * 512u + (p & 3) * 128u);
  };
  auto write_img = [&](const f32x4 (&acc)[4][5], unsigned base, const float (&sc)[5]) {
#pragma unroll
    for (int t = 0; t < 5; ++t) {
      u32x4 lo, hi;
#pragma unroll
      for (int r = 0; r < 2; ++r) {
        lo[2 * r] = cvtpk(acc[0][t][r] * sc[t], acc[1][t][r] * sc[t]); lo[2 * r + 1] = cvtpk(acc[2][t][r] * sc[t], acc[3][t][r] * sc[t]);
        hi[2 * r] = cvtpk(acc[0][t][r + 2] * sc[t], acc[1][t][r + 2] * sc[t]); hi[2 * r + 1] = cvtpk(acc[2][t][r + 2] * sc[t], acc[3][t][r + 2] * sc[t]);
      }
      *(u32x4*)(smem + base + wr0 + t * 2048u) = lo;
      *(u32x4*)(smem + base + (wr0 ^ 16u) + t * 2048u) = hi;
    }
  };
  auto oproj_store = [&](VSet (&vs)[8], unsigned u) {
    f32x4 acc[4][5];
#pragma unroll
    for (int e = 0; e < 4; ++e)
#pragma unroll
      for (int t = 0; t < 5; ++t) acc[e][t] = f32x4{0.f, 0.f, 0.f, 0.f};
#pragma unroll
    for (int ks = 0; ks < 8; ++ks) {
      u32x4 bf[5];
      load_b(bf, LDS_O + (ks >> 1) * IMG, ks & 1);
      vs_consume(vs[ks], bf, acc);
    }
    unsigned short* pb = part + ((size_t)h * (NB * SQ) + row0) * DM + u * 64u + g * 16u;
#pragma unroll
    for (int t = 0; t < 5; ++t) {
      u32x4 lo, hi;
#pragma unroll
      for (int r = 0; r < 2; ++r) {
        lo[2 * r] = cvtpk(acc[0][t][r], acc[1][t][r]); lo[2 * r + 1] = cvtpk(acc[2][t][r], acc[3][t][r]);
        hi[2 * r] = cvtpk(acc[0][t][r + 2], acc[1][t][r + 2]); hi[2 * r + 1] = cvtpk(acc[2][t][r + 2], acc[3][t][r + 2]);
      }
      const unsigned row = t * 16u + c;
      if (row < ROWS) {
        *(u32x4*)(pb + (size_t)row * DM) = lo;
        *(u32x4*)(pb + (size_t)row * DM + 8) = hi;
      }
    }
  };

  if (wv < 4) {
    __builtin_amdgcn_s_setprio(1);
    u32x4 qf[5][8];
    {
      const __amdgpu_buffer_rsrc_t srdWq = __builtin_amdgcn_make_buffer_rsrc((void*)wq, (short)0, DM * HQ * DH * 2, 0x00020000);
      const unsigned voffW = g * (8u * 8192u) + c * 8u, sW = h * 512u + w * 128u;
      VSet vs[8];
#pragma unroll
      for (int ks = 0; ks < 8; ++ks) vs_issue(vs[ks], srdWq, voffW, sW + ks * (32u * 8192u), 8192u);
      f32x4 acc[4][5];
#pragma unroll
      for (int e = 0; e < 4; ++e)
#pragma unroll
        for (int t = 0; t < 5; ++t) acc[e][t] = f32x4{0.f, 0.f, 0.f, 0.f};
      barrier();
#pragma unroll
      for (int ks = 0; ks < 14; ++ks) {
        u32x4 bf[5];
        load_b(bf, LDS_X + (ks >> 1) * IMG, ks & 1);
        vs_consume(vs[ks & 7], bf, acc);
        if (ks + 8 < 14) vs_issue(vs[ks & 7], srdWq, voffW, sW + (ks + 8) * (32u * 8192u), 8192u);
      }
      barrier();
      const float one[5] = {1.f, 1.f, 1.f, 1.f, 1.f};
      write_img(acc, LDS_Q + w * IMG, one);
      kdma(2, 2);
      barrier();
#pragma unroll
      for (int s = 0; s < 8; ++s)
#pragma unroll
        for (int t = 0; t < 5; ++t) qf[t][s] = *(const u32x4*)(smem + LDS_Q + (s >> 1) * IMG + (rd0 ^ ((s & 1) * 64u)) + t * 2048u);
    }
    const unsigned krd = LDS_K + w * 8192u + (c >> 3) * 4096u + (c & 7u) * 128u + ((g ^ (c & 7u)) << 4);
    const unsigned pwr = LDS_P + c * 128u + (((2u * w + (g >> 1)) ^ (c & 7u)) << 4) + (g & 1u) * 8u;
    float negm[5], lsum[5];
    u32x4 kf[8];
    auto kread = [&](unsigned slot) {
#pragma unroll
      for (int s = 0; s < 8; ++s) kf[s] = *(const u32x4*)(smem + (krd ^ ((s & 1) * 64u)) + slot * 32768u + (s >> 1) * 1024u);
    };
    auto expwrite = [&](const f32x4& s, int t, unsigned pbuf) {
      const float p0 = __builtin_amdgcn_exp2f(fmaf(s[0], CEXP, negm[t])), p1 = __builtin_amdgcn_exp2f(fmaf(s[1], CEXP, negm[t]));
      const float p2 = __builtin_amdgcn_exp2f(fmaf(s[2], CEXP, negm[t])), p3 = __builtin_amdgcn_exp2f(fmaf(s[3], CEXP, negm[t]));
      lsum[t] += (p0 + p1) + (p2 + p3);
      u32x2 pk = {cvtpk(p0, p1), cvtpk(p2, p3)};
      *(u32x2*)(smem + pwr + pbuf * IMG + t * 2048u) = pk;
    };
    {
      kread(0);
      f32x4 s0[5];
#pragma unroll
      for (int t = 0; t < 5; ++t) {
        f32x4 s = {0.f, 0.f, 0.f, 0.f};
#pragma unroll
        for (int k = 0; k < 8; ++k) s = mfma(kf[k], qf[t][k], s);
        s0[t] = s;
        float m = fmaxf(fmaxf(s[0], s[1]), fmaxf(s[2], s[3]));
        m = fmaxf(m, __shfl_xor(m, 16)); m = fmaxf(m, __shfl_xor(m, 32));
        if (g == 0) misc[w * 80u + t * 16u + c] = m;
      }
      barrier();
      kdma(3, 3);
      kdma(4, 0);
#pragma unroll
      for (int t = 0; t < 5; ++t) {
        const float m = fmaxf(fmaxf(misc[t * 16u + c], misc[80u + t * 16u + c]), fmaxf(misc[160u + t * 16u + c], misc[240u + t * 16u + c]));
        negm[t] = -m * CEXP; lsum[t] = 0.f;
        expwrite(s0[t], t, 0);
      }
      barrier();
    }
#pragma unroll 1
    for (int it = 0; it < 4; ++it) {
#pragma unroll
      for (int q = 0; q < 4; ++q) {
        const unsigned n = it * 4 + q + 1;
        if (q < 3 || it < 3) {
          asm volatile("s_waitcnt vmcnt(24)" ::: "memory");
          kread((q + 1) & 3);
          asm volatile("s_waitcnt lgkmcnt(0)" ::: "memory");
          kdma(n + 4, (q + 1) & 3);
#pragma unroll
          for (int t = 0; t < 5; ++t) {
            f32x4 s = {0.f, 0.f, 0.f, 0.f};
#pragma unroll
            for (int k = 0; k < 8; ++k) s = mfma(kf[k], qf[t][k], s);
            expwrite(s, t, (q + 1) & 1);
          }
        } else {
#pragma unroll
          for (int t = 0; t < 5; ++t) {
            float l = lsum[t];
            l += __shfl_xor(l, 16); l += __shfl_xor(l, 32);
            if (g == 0) misc[320u + w * 80u + t * 16u + c] = l;
          }
          asm volatile("s_waitcnt vmcnt(0)" ::: "memory");
        }
        if (q < 3 || it < 3) barrier();
      }
    }
    VSet vs[8];
    const unsigned voffO = g * (8u * 896u) + c * 8u;
#pragma unroll
    for (int ks = 0; ks < 8; ++ks) vs_issue(vs[ks], srdWo, voffO, w * 128u + ks * (32u * 896u), 896u);
    barrier();
    barrier();
    oproj_store(vs, w);
  } else {
    const __amdgpu_buffer_rsrc_t srdV = __builtin_amdgcn_make_buffer_rsrc((void*)(vc + kvoff), (short)0, SK * DH * 2, 0x00020000);
    {
      const __amdgpu_buffer_rsrc_t srdX = __builtin_amdgcn_make_buffer_rsrc((void*)x, (short)0, NB * SQ * DM * 2, 0x00020000);
      const unsigned voffX = r8 * 896u + swz;
#pragma unroll
      for (int j = 0; j < 18; ++j) {
        unsigned p = w + 4u * j; p = p < 70u ? p : 69u;
        const unsigned kb = p / 10u, rg = p - kb * 10u;
        dma16(ldsb + LDS_X + kb * IMG + rg * 1024u, voffX, srdX, (row0 + rg * 8u) * 896u + kb * 128u);
      }
    }
    kdma(0, 0);
    kdma(1, 1);
    VSet vs[8];
    const unsigned voffV = g * (8u * 512u) + c * 8u, voffO = g * (8u * 896u) + c * 8u;
#pragma unroll
    for (int ks = 0; ks < 3; ++ks) vs_issue(vs[ks], srdV, voffV, w * 128u + ks * 16384u, 512u);
    asm volatile("s_waitcnt vmcnt(40)" ::: "memory");
    barrier();
#pragma unroll
    for (int ks = 3; ks < 4; ++ks) vs_issue(vs[ks], srdV, voffV, w * 128u + ks * 16384u, 512u);
    barrier();
    asm volatile("s_waitcnt vmcnt(32)" ::: "memory");
    barrier();
    barrier();
    f32x4 acc[4][5];
#pragma unroll
    for (int e = 0; e < 4; ++e)
#pragma unroll
      for (int t = 0; t < 5; ++t) acc[e][t] = f32x4{0.f, 0.f, 0.f, 0.f};
    barrier();
#pragma unroll 1
    for (int it = 0; it < 8; ++it) {
      const unsigned sb = w * 128u + (it + 1) * (4u * 16384u);
#pragma unroll
      for (int q = 0; q < 2; ++q) {
#pragma unroll
        for (int sh = 0; sh < 2; ++sh) {
          u32x4 bf[5];
          load_b(bf, LDS_P + (q & 1) * IMG, sh);
          vs_consume(vs[2 * q + sh], bf, acc);
          vs_issue(vs[2 * q + sh], srdV, voffV, sb + (2 * q + sh) * 16384u, 512u);
        }
        barrier();
      }
    }
#pragma unroll
    for (int ks = 0; ks < 8; ++ks) vs_issue(vs[ks], srdWo, voffO, (4u + w) * 128u + ks * (32u * 896u), 896u);
    float rinv[5];
#pragma unroll
    for (int t = 0; t < 5; ++t) {
      const float l = (misc[320u + t * 16u + c] + misc[400u + t * 16u + c]) + (misc[480u + t * 16u + c] + misc[560u + t * 16u + c]);
      rinv[t] = l <= LBIG ? 1.f / l : __builtin_nanf("");
    }
    write_img(acc, LDS_O + w * IMG, rinv);
    barrier();
    if (w < 3) oproj_store(vs, 4u + w);
  }
}

__device__ __forceinline__ float bf2f(unsigned short v) { return __uint_as_float((unsigned)v << 16); }
__device__ __forceinline__ float rbf(float v) { return (float)(__bf16)v; }

__global__ __launch_bounds__(256) void gqa_reduce(const unsigned short* __restrict__ part, unsigned short* __restrict__ out,
                                                  const unsigned short* __restrict__ x, const unsigned short* __restrict__ kc,
                                                  const unsigned short* __restrict__ vc, const unsigned short* __restrict__ wq,
                                                  const unsigned short* __restrict__ wo) {
  __shared__ float sbuf[4][SK + 2 * DH];
  const unsigned lane = threadIdx.x & 63u, wv = threadIdx.x >> 6, row = blockIdx.x * 4 + wv;
  float s[8] = {0.f, 0.f, 0.f, 0.f, 0.f, 0.f, 0.f, 0.f};
  if (lane < 56) {
    u32x4 v[HQ];
#pragma unroll
    for (int h = 0; h < HQ; ++h) v[h] = *(const u32x4*)(part + ((size_t)h * (NB * SQ) + row) * DM + lane * 8u);
#pragma unroll
    for (int h = 0; h < HQ; ++h)
#pragma unroll
      for (int j = 0; j < 4; ++j) { s[2 * j] += __uint_as_float(v[h][j] << 16); s[2 * j + 1] += __uint_as_float(v[h][j] & 0xffff0000u); }
  }
  bool bad = false;
#pragma unroll
  for (int j = 0; j < 8; ++j) bad |= !(fabsf(s[j]) <= 3.0e38f);
  if (__any(bad)) {
    float* sc = sbuf[wv]; float* qv = sc + SK; float* ov = qv + DH;
    const unsigned b = row / SQ;
#pragma unroll
    for (int j = 0; j < 8; ++j) s[j] = 0.f;
    for (int h = 0; h < HQ; ++h) {
      const unsigned short* K = kc + (size_t)(b * HKV + h / 2) * SK * DH;
      const unsigned short* V = vc + (size_t)(b * HKV + h / 2) * SK * DH;
      for (int d = lane; d < DH; d += 64) {
        float a = 0.f;
        for (int k = 0; k < DM; ++k) a = fmaf(bf2f(x[(size_t)row * DM + k]), bf2f(wq[(size_t)k * (HQ * DH) + h * DH + d]), a);
        qv[d] = rbf(a);
      }
      __builtin_amdgcn_fence(__ATOMIC_SEQ_CST, "wavefront");
      float m = -__builtin_inff();
      for (int j = lane; j < SK; j += 64) {
        float a = 0.f;
        for (int d = 0; d < DH; ++d) a = fmaf(qv[d], bf2f(K[(size_t)j * DH + d]), a);
        sc[j] = a; m = fmaxf(m, a);
      }
      for (int o = 32; o > 0; o >>= 1) m = fmaxf(m, __shfl_xor(m, o));
      float l = 0.f;
      for (int j = lane; j < SK; j += 64) { const float p = exp2f((sc[j] - m) * CEXP); sc[j] = p; l += p; }
      for (int o = 32; o > 0; o >>= 1) l += __shfl_xor(l, o);
      __builtin_amdgcn_fence(__ATOMIC_SEQ_CST, "wavefront");
      for (int d = lane; d < DH; d += 64) {
        float a = 0.f;
        for (int j = 0; j < SK; ++j) a = fmaf(sc[j], bf2f(V[(size_t)j * DH + d]), a);
        ov[d] = rbf(a / l);
      }
      __builtin_amdgcn_fence(__ATOMIC_SEQ_CST, "wavefront");
      if (lane < 56)
        for (int d = 0; d < DH; ++d) {
          const float o = ov[d];
#pragma unroll
          for (int j = 0; j < 8; ++j) s[j] = fmaf(o, bf2f(wo[(size_t)(h * DH + d) * DM + lane * 8u + j]), s[j]);
        }
      __builtin_amdgcn_fence(__ATOMIC_SEQ_CST, "wavefront");
    }
  }
  if (lane < 56) {
    u32x4 o = {cvtpk(s[0], s[1]), cvtpk(s[2], s[3]), cvtpk(s[4], s[5]), cvtpk(s[6], s[7])};
    *(u32x4*)(out + (size_t)row * DM + lane * 8u) = o;
  }
}

extern "C" void kernel_launch(void* const* d_in, const int* in_sizes, int n_in, void* d_out, int out_size, void* d_ws, size_t ws_size,
                              hipStream_t stream) {
  const unsigned short* x = (const unsigned short*)d_in[0];
  const unsigned short* kc = (const unsigned short*)d_in[1];
  const unsigned short* vc = (const unsigned short*)d_in[2];
  const unsigned short* wq = (const unsigned short*)d_in[3];
  const unsigned short* wo = (const unsigned short*)d_in[4];
  unsigned short* part = (unsigned short*)d_ws;
  (void)hipFuncSetAttribute((const void*)gqa_main, hipFuncAttributeMaxDynamicSharedMemorySize, (int)LDS_BYTES);
  gqa_main<<<256, 512, LDS_BYTES, stream>>>(x, kc, vc, wq, wo, part);
  gqa_reduce<<<NB * SQ / 4, 256, 0, stream>>>(part, (unsigned short*)d_out, x, kc, vc, wq, wo);
}
```

```cpp
#include <hip/hip_runtime.h>
typedef unsigned short us;
__device__ float b2f(us v){ return __uint_as_float((unsigned)v<<16);}
__global__ __launch_bounds__(256) void one(const us* x,const us* kc,const us* vc,const us* wq,const us* wo,us* out){
  const unsigned long long t0=__builtin_amdgcn_s_memrealtime();
  __shared__ float qs[256], s[1024], red[256], ov[256];
  int t=threadIdx.x;
  for(int m=blockIdx.x;m<1152;m+=gridDim.x){ int b=m/144; float a0=0,a1=0;
    for(int h=0;h<16;++h){
      float a=0; for(int k=0;k<448;++k) a+=b2f(x[m*448+k])*b2f(wq[(size_t)k*4096+h*256+t]); qs[t]=(float)(__bf16)a; __syncthreads();
      const us* K=kc+((size_t)(b*8+h/2))*1024*256; const us* V=vc+((size_t)(b*8+h/2))*1024*256; float mx=-1e30f;
      for(int j=t;j<1024;j+=256){ float c=0; for(int d=0;d<256;++d) c+=qs[d]*b2f(K[(size_t)j*256+d]); c*=0.0625f; s[j]=c; mx=fmaxf(mx,c);}
      red[t]=mx; __syncthreads(); for(int o=128;o>0;o>>=1){ if(t<o) red[t]=fmaxf(red[t],red[t+o]); __syncthreads(); } mx=red[0]; __syncthreads();
      float sum=0; for(int j=t;j<1024;j+=256){ float e=__expf(s[j]-mx); s[j]=e; sum+=e; } red[t]=sum; __syncthreads(); for(int o=128;o>0;o>>=1){ if(t<o) red[t]+=red[t+o]; __syncthreads(); } sum=red[0];
      float c=0; for(int j=0;j<1024;++j) c+=s[j]*b2f(V[(size_t)j*256+t]); ov[t]=(float)(__bf16)(c/sum); __syncthreads();
      for(int d=0;d<256;++d){ float o=ov[d]; a0+=o*b2f(wo[(size_t)(h*256+d)*448+t]); if(t<192) a1+=o*b2f(wo[(size_t)(h*256+d)*448+256+t]); }
      __syncthreads(); }
    __bf16 r0=(__bf16)a0, r1=(__bf16)a1; out[m*448+t]=__builtin_bit_cast(us,r0); if(t<192) out[m*448+256+t]=__builtin_bit_cast(us,r1); }
  if(blockIdx.x==0) while(__builtin_amdgcn_s_memrealtime()-t0<1000000ull) __builtin_amdgcn_s_sleep(1);
}
__global__ void tail(char* ws){ if(threadIdx.x==0) *(volatile unsigned*)ws=1u; }
extern "C" void kernel_launch(void* const* d_in, const int* in_sizes, int n_in, void* d_out, int out_size, void* d_ws, size_t ws_size, hipStream_t stream){
  one<<<1152,256,0,stream>>>((const us*)d_in[0],(const us*)d_in[1],(const us*)d_in[2],(const us*)d_in[3],(const us*)d_in[4],(us*)d_out);
}
```

```cpp
#include <hip/hip_runtime.h>
#include <hip/hip_bf16.h>

typedef __bf16 bf16x2 __attribute__((ext_vector_type(2)));
typedef float f32x2 __attribute__((ext_vector_type(2)));
typedef unsigned u32x4 __attribute__((ext_vector_type(4)));
typedef unsigned u32x2 __attribute__((ext_vector_type(2)));
#define DI __device__ __forceinline__

namespace {
constexpr int NB = 8, SQ = 144, DM = 448, HQ = 16, HKV = 8, SK = 1024, DH = 256;
constexpr unsigned LDS_BYTES = 163840;
constexpr float CEXP = 0.09016844005556021f;
constexpr size_t PART_WG = 20 * 3584;
constexpr size_t PART_OFF = (size_t)2 << 20;
DI unsigned cvtpk(float lo, float hi) { f32x2 v = {lo, hi}; bf16x2 b = __builtin_convertvector(v, bf16x2); return __builtin_bit_cast(unsigned, b); }
DI float bf2f(unsigned short v) { return __uint_as_float((unsigned)v << 16); }
DI float rbf(float v) { return (float)(__bf16)v; }
}

typedef __attribute__((address_space(1))) unsigned gu32;
DI unsigned ld_sc1(const unsigned* p) { return __hip_atomic_load((const gu32*)p, __ATOMIC_RELAXED, __HIP_MEMORY_SCOPE_AGENT); }
DI void st_sc1(unsigned* p, unsigned v) { __hip_atomic_store((gu32*)p, v, __ATOMIC_RELAXED, __HIP_MEMORY_SCOPE_AGENT); }
DI unsigned lane_id() { return __builtin_amdgcn_mbcnt_hi(~0u, __builtin_amdgcn_mbcnt_lo(~0u, 0u)); }

struct Args { const unsigned short *x, *kc, *vc, *wq, *wo; char* ws; unsigned short* out; };
typedef const __attribute__((address_space(4))) Args* KArgs;

__global__ __launch_bounds__(512, 2) void gqa_main(const unsigned short* __restrict__ x, const unsigned short* __restrict__ kc,
                                                   const unsigned short* __restrict__ vc, const unsigned short* __restrict__ wq,
                                                   const unsigned short* __restrict__ wo, char* __restrict__ ws, unsigned short* __restrict__ out) {
  asm volatile(
      "s_getpc_b64 s[48:49]\n"
      "Lgqa_pc:\n"
      "s_and_b32 s49, s49, 0xffff\n"
      "s_mov_b32 s50, Lgqa_body_end-Lgqa_pc+6000\n"
      "s_mov_b32 s51, 0x00020000\n"
      "s_lshr_b32 s52, %1, 3\n"
      "s_lshl_b32 s52, s52, 11\n"
      "v_lshlrev_b32 v120, 7, %0\n"
      "v_cmp_gt_u32 vcc, 16, %0\n"
      "s_and_saveexec_b64 s[54:55], vcc\n"
      "s_cbranch_execz Lgqa_nopf\n"
      "buffer_load_dword v121, v120, s[48:51], s52 offen\n"
      "Lgqa_nopf:\n"
      "s_mov_b64 exec, s[54:55]\n"
      :: "v"(threadIdx.x), "s"(blockIdx.x) : "s48", "s49", "s50", "s51", "s52", "s54", "s55", "v120", "v121", "vcc", "scc", "memory");
  extern __shared__ __attribute__((aligned(16))) char smem[];
  const unsigned wv = __builtin_amdgcn_readfirstlane(threadIdx.x >> 6);
  unsigned bid = blockIdx.x;
  KArgs ka = (KArgs)__builtin_amdgcn_kernarg_segment_ptr();
  unsigned cs;
  {
  const unsigned kvh = bid & 7u, jj = bid >> 3, b = jj >> 2, br = jj >> 1, hh = jj & 1u, h = kvh * 2 + hh;
  unsigned* chal = (unsigned*)(ws + br * 256u);
  unsigned* echo = (unsigned*)(ws + 65536 + br * 2048u);
  if (wv == 0) {
    const unsigned old = ld_sc1(echo + h * 32u + (threadIdx.x & 15u));
    unsigned c0 = __builtin_amdgcn_readfirstlane(old) + 1u;
    while (__any(old == c0)) ++c0;
    if (threadIdx.x == 0) st_sc1(chal + h, c0);
  }
  {
    const size_t kvoff = (size_t)(b * HKV + kvh) * SK * DH;
    const unsigned short* pk = kc + kvoff;
    const unsigned short* pv = vc + kvoff;
    const unsigned short* px = x + (size_t)br * 72 * DM;
    const unsigned short* pwq = wq + h * DH;
    const unsigned nwq = DM * HQ * DH * 2 - h * DH * 2;
    const unsigned rot = (br + 3u * hh) % 7u;
    const unsigned sub = 0u;
    const unsigned short* pwo = wo + (size_t)h * DH * DM;
    char* ppart = ws + PART_OFF + ((size_t)h * 16 + br) * PART_WG;
    asm volatile(
      "s_nop 4\n"
      "s_mov_b32 s64, %[wv]\n"
      "s_and_b32 s52, s64, 3\n"
      "v_mbcnt_lo_u32_b32 v251, -1, 0\n"
      "v_mbcnt_hi_u32_b32 v251, -1, v251\n"
      "v_lshrrev_b32 v252, 4, v251\n"
      "v_and_b32 v253, 15, v251\n"
      "v_and_b32 v254, 7, v251\n"
      "s_mov_b32 s87, %[rot]\n"
      "s_add_u32 s88, s87, 1\n"
      "s_cmp_eq_u32 s88, 7\n"
      "s_cselect_b32 s88, 0, s88\n"
      "s_add_u32 s89, s88, 1\n"
      "s_cmp_eq_u32 s89, 7\n"
      "s_cselect_b32 s89, 0, s89\n"
      "s_add_u32 s90, s89, 1\n"
      "s_cmp_eq_u32 s90, 7\n"
      "s_cselect_b32 s90, 0, s90\n"
      "s_add_u32 s91, s90, 1\n"
      "s_cmp_eq_u32 s91, 7\n"
      "s_cselect_b32 s91, 0, s91\n"
      "s_add_u32 s92, s91, 1\n"
      "s_cmp_eq_u32 s92, 7\n"
      "s_cselect_b32 s92, 0, s92\n"
      "s_add_u32 s93, s92, 1\n"
      "s_cmp_eq_u32 s93, 7\n"
      "s_cselect_b32 s93, 0, s93\n"
      "s_cmp_lt_u32 s64, 4\n"
      "s_cbranch_scc1 LQKR%=\n"
      "s_mov_b64 s[48:49], %[px]\n"
      "s_and_b32 s49, s49, 0xffff\n"
      "s_mov_b32 s50, 64512\n"
      "s_mov_b32 s51, 0x00020000\n"
      "v_lshrrev_b32 v255, 3, v251\n"
      "v_xor_b32 v238, v255, v254\n"
      "v_lshlrev_b32 v238, 4, v238\n"
      "v_mul_u32_u24 v255, 896, v255\n"
      "v_add_u32 v238, v255, v238\n"
      "s_lshl_b32 s62, s52, 10\n"
      "s_mul_i32 s69, s52, 7168\n"
      "s_mov_b64 s[40:41], %[pwq]\n"
      "s_and_b32 s41, s41, 0xffff\n"
      "s_mov_b32 s42, %[nwq]\n"
      "s_mov_b32 s43, 0x00020000\n"
      "v_bfe_u32 v250, v251, 4, 1\n"
      "s_and_b32 s57, s52, 1\n"
      "s_lshl_b32 s57, s57, 1\n"
      "v_or_b32 v250, s57, v250\n"
      "v_lshlrev_b32 v250, 1, v250\n"
      "v_xor_b32 v250, v250, v254\n"
      "v_lshlrev_b32 v250, 4, v250\n"
      "v_lshrrev_b32 v255, 3, v251\n"
      "v_mul_u32_u24 v255, 8192, v255\n"
      "v_add_u32 v250, v255, v250\n"
      "s_lshl_b32 s60, s52, 12\n"
      "s_lshl_b32 s59, s52, 16\n"
      "s_lshl_b32 s63, s87, 7\n"
      "s_add_u32 s63, s63, s69\n"
      "s_cmp_gt_u32 s52, 0\n"
      "s_cbranch_scc1 LX1%=\n"
      "s_add_u32 m0, s62, 106496\n"
      "s_add_u32 s57, s63, 57344\n"
      "buffer_load_dwordx4 v238, s[48:51], s57 offen lds\n"
      "LX1%=:\n"
      "s_add_u32 m0, s62, 98304\n"
      "s_add_u32 s57, s63, 0\n"
      "buffer_load_dwordx4 v238, s[48:51], s57 offen lds\n"
      "s_add_u32 m0, s62, 102400\n"
      "s_add_u32 s57, s63, 28672\n"
      "buffer_load_dwordx4 v238, s[48:51], s57 offen lds\n"
      "s_lshl_b32 s61, s87, 19\n"
      "s_add_u32 s61, s61, s59\n"
      "s_add_u32 m0, s60, 65536\n"
      "s_add_u32 s57, s61, 0\n"
      "buffer_load_dwordx4 v250, s[40:43], s57 offen lds\n"
      "s_add_u32 m0, s60, 66560\n"
      "s_add_u32 s57, s61, 128\n"
      "buffer_load_dwordx4 v250, s[40:43], s57 offen lds\n"
      "s_add_u32 m0, s60, 67584\n"
      "s_add_u32 s57, s61, 256\n"
      "buffer_load_dwordx4 v250, s[40:43], s57 offen lds\n"
      "s_add_u32 m0, s60, 68608\n"
      "s_add_u32 s57, s61, 384\n"
      "buffer_load_dwordx4 v250, s[40:43], s57 offen lds\n"
      "s_lshl_b32 s61, s87, 19\n"
      "s_add_u32 s61, s61, s59\n"
      "s_add_u32 m0, s60, 81920\n"
      "s_add_u32 s57, s61, 262144\n"
      "buffer_load_dwordx4 v250, s[40:43], s57 offen lds\n"
      "s_add_u32 m0, s60, 82944\n"
      "s_add_u32 s57, s61, 262272\n"
      "buffer_load_dwordx4 v250, s[40:43], s57 offen lds\n"
      "s_add_u32 m0, s60, 83968\n"
      "s_add_u32 s57, s61, 262400\n"
      "buffer_load_dwordx4 v250, s[40:43], s57 offen lds\n"
      "s_add_u32 m0, s60, 84992\n"
      "s_add_u32 s57, s61, 262528\n"
      "buffer_load_dwordx4 v250, s[40:43], s57 offen lds\n"
      "s_lshl_b32 s63, s88, 7\n"
      "s_add_u32 s63, s63, s69\n"
      "s_cmp_gt_u32 s52, 0\n"
      "s_cbranch_scc1 LX2%=\n"
      "s_add_u32 m0, s62, 115712\n"
      "s_add_u32 s57, s63, 57344\n"
      "buffer_load_dwordx4 v238, s[48:51], s57 offen lds\n"
      "LX2%=:\n"
      "s_add_u32 m0, s62, 107520\n"
      "s_add_u32 s57, s63, 0\n"
      "buffer_load_dwordx4 v238, s[48:51], s57 offen lds\n"
      "s_add_u32 m0, s62, 111616\n"
      "s_add_u32 s57, s63, 28672\n"
      "buffer_load_dwordx4 v238, s[48:51], s57 offen lds\n"
      "s_lshl_b32 s61, s88, 19\n"
      "s_add_u32 s61, s61, s59\n"
      "s_add_u32 m0, s60, 0\n"
      "s_add_u32 s57, s61, 0\n"
      "buffer_load_dwordx4 v250, s[40:43], s57 offen lds\n"
      "s_add_u32 m0, s60, 1024\n"
      "s_add_u32 s57, s61, 128\n"
      "buffer_load_dwordx4 v250, s[40:43], s57 offen lds\n"
      "s_add_u32 m0, s60, 2048\n"
      "s_add_u32 s57, s61, 256\n"
      "buffer_load_dwordx4 v250, s[40:43], s57 offen lds\n"
      "s_add_u32 m0, s60, 3072\n"
      "s_add_u32 s57, s61, 384\n"
      "buffer_load_dwordx4 v250, s[40:43], s57 offen lds\n"
      "s_lshl_b32 s63, s89, 7\n"
      "s_add_u32 s63, s63, s69\n"
      "s_cmp_gt_u32 s52, 0\n"
      "s_cbranch_scc1 LX3%=\n"
      "s_add_u32 m0, s62, 124928\n"
      "s_add_u32 s57, s63, 57344\n"
      "buffer_load_dwordx4 v238, s[48:51], s57 offen lds\n"
      "LX3%=:\n"
      "s_add_u32 m0, s62, 116736\n"
      "s_add_u32 s57, s63, 0\n"
      "buffer_load_dwordx4 v238, s[48:51], s57 offen lds\n"
      "s_add_u32 m0, s62, 120832\n"
      "s_add_u32 s57, s63, 28672\n"
      "buffer_load_dwordx4 v238, s[48:51], s57 offen lds\n"
      "s_lshl_b32 s61, s88, 19\n"
      "s_add_u32 s61, s61, s59\n"
      "s_add_u32 m0, s60, 16384\n"
      "s_add_u32 s57, s61, 262144\n"
      "buffer_load_dwordx4 v250, s[40:43], s57 offen lds\n"
      "s_add_u32 m0, s60, 17408\n"
      "s_add_u32 s57, s61, 262272\n"
      "buffer_load_dwordx4 v250, s[40:43], s57 offen lds\n"
      "s_add_u32 m0, s60, 18432\n"
      "s_add_u32 s57, s61, 262400\n"
      "buffer_load_dwordx4 v250, s[40:43], s57 offen lds\n"
      "s_add_u32 m0, s60, 19456\n"
      "s_add_u32 s57, s61, 262528\n"
      "buffer_load_dwordx4 v250, s[40:43], s57 offen lds\n"
      "s_lshl_b32 s61, s89, 19\n"
      "s_add_u32 s61, s61, s59\n"
      "s_add_u32 m0, s60, 32768\n"
      "s_add_u32 s57, s61, 0\n"
      "buffer_load_dwordx4 v250, s[40:43], s57 offen lds\n"
      "s_add_u32 m0, s60, 33792\n"
      "s_add_u32 s57, s61, 128\n"
      "buffer_load_dwordx4 v250, s[40:43], s57 offen lds\n"
      "s_add_u32 m0, s60, 34816\n"
      "s_add_u32 s57, s61, 256\n"
      "buffer_load_dwordx4 v250, s[40:43], s57 offen lds\n"
      "s_add_u32 m0, s60, 35840\n"
      "s_add_u32 s57, s61, 384\n"
      "buffer_load_dwordx4 v250, s[40:43], s57 offen lds\n"
      "s_waitcnt vmcnt(10)\n"
      "s_waitcnt lgkmcnt(0)\n"
      "s_barrier\n"
      "s_lshl_b32 s63, s90, 7\n"
      "s_add_u32 s63, s63, s69\n"
      "s_cmp_gt_u32 s52, 0\n"
      "s_cbranch_scc1 LX4%=\n"
      "s_add_u32 m0, s62, 134144\n"
      "s_add_u32 s57, s63, 57344\n"
      "buffer_load_dwordx4 v238, s[48:51], s57 offen lds\n"
      "LX4%=:\n"
      "s_add_u32 m0, s62, 125952\n"
      "s_add_u32 s57, s63, 0\n"
      "buffer_load_dwordx4 v238, s[48:51], s57 offen lds\n"
      "s_add_u32 m0, s62, 130048\n"
      "s_add_u32 s57, s63, 28672\n"
      "buffer_load_dwordx4 v238, s[48:51], s57 offen lds\n"
      "s_lshl_b32 s61, s89, 19\n"
      "s_add_u32 s61, s61, s59\n"
      "s_add_u32 m0, s60, 49152\n"
      "s_add_u32 s57, s61, 262144\n"
      "buffer_load_dwordx4 v250, s[40:43], s57 offen lds\n"
      "s_add_u32 m0, s60, 50176\n"
      "s_add_u32 s57, s61, 262272\n"
      "buffer_load_dwordx4 v250, s[40:43], s57 offen lds\n"
      "s_add_u32 m0, s60, 51200\n"
      "s_add_u32 s57, s61, 262400\n"
      "buffer_load_dwordx4 v250, s[40:43], s57 offen lds\n"
      "s_add_u32 m0, s60, 52224\n"
      "s_add_u32 s57, s61, 262528\n"
      "buffer_load_dwordx4 v250, s[40:43], s57 offen lds\n"
      "s_lshl_b32 s63, s91, 7\n"
      "s_add_u32 s63, s63, s69\n"
      "s_cmp_gt_u32 s52, 0\n"
      "s_cbranch_scc1 LX5%=\n"
      "s_add_u32 m0, s62, 143360\n"
      "s_add_u32 s57, s63, 57344\n"
      "buffer_load_dwordx4 v238, s[48:51], s57 offen lds\n"
      "LX5%=:\n"
      "s_add_u32 m0, s62, 135168\n"
      "s_add_u32 s57, s63, 0\n"
      "buffer_load_dwordx4 v238, s[48:51], s57 offen lds\n"
      "s_add_u32 m0, s62, 139264\n"
      "s_add_u32 s57, s63, 28672\n"
      "buffer_load_dwordx4 v238, s[48:51], s57 offen lds\n"
      "s_waitcnt vmcnt(8)\n"
      "s_waitcnt lgkmcnt(0)\n"
      "s_barrier\n"
      "s_lshl_b32 s61, s90, 19\n"
      "s_add_u32 s61, s61, s59\n"
      "s_add_u32 m0, s60, 65536\n"
      "s_add_u32 s57, s61, 0\n"
      "buffer_load_dwordx4 v250, s[40:43], s57 offen lds\n"
      "s_add_u32 m0, s60, 66560\n"
      "s_add_u32 s57, s61, 128\n"
      "buffer_load_dwordx4 v250, s[40:43], s57 offen lds\n"
      "s_add_u32 m0, s60, 67584\n"
      "s_add_u32 s57, s61, 256\n"
      "buffer_load_dwordx4 v250, s[40:43], s57 offen lds\n"
      "s_add_u32 m0, s60, 68608\n"
      "s_add_u32 s57, s61, 384\n"
      "buffer_load_dwordx4 v250, s[40:43], s57 offen lds\n"
      "s_lshl_b32 s61, s90, 19\n"
      "s_add_u32 s61, s61, s59\n"
      "s_add_u32 m0, s60, 81920\n"
      "s_add_u32 s57, s61, 262144\n"
      "buffer_load_dwordx4 v250, s[40:43], s57 offen lds\n"
      "s_add_u32 m0, s60, 82944\n"
      "s_add_u32 s57, s61, 262272\n"
      "buffer_load_dwordx4 v250, s[40:43], s57 offen lds\n"
      "s_add_u32 m0, s60, 83968\n"
      "s_add_u32 s57, s61, 262400\n"
      "buffer_load_dwordx4 v250, s[40:43], s57 offen lds\n"
      "s_add_u32 m0, s60, 84992\n"
      "s_add_u32 s57, s61, 262528\n"
      "buffer_load_dwordx4 v250, s[40:43], s57 offen lds\n"
      "s_lshl_b32 s61, s91, 19\n"
      "s_add_u32 s61, s61, s59\n"
      "s_add_u32 m0, s60, 0\n"
      "s_add_u32 s57, s61, 0\n"
      "buffer_load_dwordx4 v250, s[40:43], s57 offen lds\n"
      "s_add_u32 m0, s60, 1024\n"
      "s_add_u32 s57, s61, 128\n"
      "buffer_load_dwordx4 v250, s[40:43], s57 offen lds\n"
      "s_add_u32 m0, s60, 2048\n"
      "s_add_u32 s57, s61, 256\n"
      "buffer_load_dwordx4 v250, s[40:43], s57 offen lds\n"
      "s_add_u32 m0, s60, 3072\n"
      "s_add_u32 s57, s61, 384\n"
      "buffer_load_dwordx4 v250, s[40:43], s57 offen lds\n"
      "s_lshl_b32 s63, s92, 7\n"
      "s_add_u32 s63, s63, s69\n"
      "s_cmp_gt_u32 s52, 0\n"
      "s_cbranch_scc1 LX6%=\n"
      "s_add_u32 m0, s62, 152576\n"
      "s_add_u32 s57, s63, 57344\n"
      "buffer_load_dwordx4 v238, s[48:51], s57 offen lds\n"
      "LX6%=:\n"
      "s_add_u32 m0, s62, 144384\n"
      "s_add_u32 s57, s63, 0\n"
      "buffer_load_dwordx4 v238, s[48:51], s57 offen lds\n"
      "s_add_u32 m0, s62, 148480\n"
      "s_add_u32 s57, s63, 28672\n"
      "buffer_load_dwordx4 v238, s[48:51], s57 offen lds\n"
      "s_waitcnt vmcnt(10)\n"
      "s_waitcnt lgkmcnt(0)\n"
      "s_barrier\n"
      "s_lshl_b32 s61, s91, 19\n"
      "s_add_u32 s61, s61, s59\n"
      "s_add_u32 m0, s60, 16384\n"
      "s_add_u32 s57, s61, 262144\n"
      "buffer_load_dwordx4 v250, s[40:43], s57 offen lds\n"
      "s_add_u32 m0, s60, 17408\n"
      "s_add_u32 s57, s61, 262272\n"
      "buffer_load_dwordx4 v250, s[40:43], s57 offen lds\n"
      "s_add_u32 m0, s60, 18432\n"
      "s_add_u32 s57, s61, 262400\n"
      "buffer_load_dwordx4 v250, s[40:43], s57 offen lds\n"
      "s_add_u32 m0, s60, 19456\n"
      "s_add_u32 s57, s61, 262528\n"
      "buffer_load_dwordx4 v250, s[40:43], s57 offen lds\n"
      "s_lshl_b32 s61, s92, 19\n"
      "s_add_u32 s61, s61, s59\n"
      "s_add_u32 m0, s60, 32768\n"
      "s_add_u32 s57, s61, 0\n"
      "buffer_load_dwordx4 v250, s[40:43], s57 offen lds\n"
      "s_add_u32 m0, s60, 33792\n"
      "s_add_u32 s57, s61, 128\n"
      "buffer_load_dwordx4 v250, s[40:43], s57 offen lds\n"
      "s_add_u32 m0, s60, 34816\n"
      "s_add_u32 s57, s61, 256\n"
      "buffer_load_dwordx4 v250, s[40:43], s57 offen lds\n"
      "s_add_u32 m0, s60, 35840\n"
      "s_add_u32 s57, s61, 384\n"
      "buffer_load_dwordx4 v250, s[40:43], s57 offen lds\n"
      "s_lshl_b32 s63, s93, 7\n"
      "s_add_u32 s63, s63, s69\n"
      "s_cmp_gt_u32 s52, 0\n"
      "s_cbranch_scc1 LX7%=\n"
      "s_add_u32 m0, s62, 161792\n"
      "s_add_u32 s57, s63, 57344\n"
      "buffer_load_dwordx4 v238, s[48:51], s57 offen lds\n"
      "LX7%=:\n"
      "s_add_u32 m0, s62, 153600\n"
      "s_add_u32 s57, s63, 0\n"
      "buffer_load_dwordx4 v238, s[48:51], s57 offen lds\n"
      "s_add_u32 m0, s62, 157696\n"
      "s_add_u32 s57, s63, 28672\n"
      "buffer_load_dwordx4 v238, s[48:51], s57 offen lds\n"
      "s_waitcnt vmcnt(12)\n"
      "s_waitcnt lgkmcnt(0)\n"
      "s_barrier\n"
      "s_lshl_b32 s61, s92, 19\n"
      "s_add_u32 s61, s61, s59\n"
      "s_add_u32 m0, s60, 49152\n"
      "s_add_u32 s57, s61, 262144\n"
      "buffer_load_dwordx4 v250, s[40:43], s57 offen lds\n"
      "s_add_u32 m0, s60, 50176\n"
      "s_add_u32 s57, s61, 262272\n"
      "buffer_load_dwordx4 v250, s[40:43], s57 offen lds\n"
      "s_add_u32 m0, s60, 51200\n"
      "s_add_u32 s57, s61, 262400\n"
      "buffer_load_dwordx4 v250, s[40:43], s57 offen lds\n"
      "s_add_u32 m0, s60, 52224\n"
      "s_add_u32 s57, s61, 262528\n"
      "buffer_load_dwordx4 v250, s[40:43], s57 offen lds\n"
      "s_lshl_b32 s61, s93, 19\n"
      "s_add_u32 s61, s61, s59\n"
      "s_add_u32 m0, s60, 65536\n"
      "s_add_u32 s57, s61, 0\n"
      "buffer_load_dwordx4 v250, s[40:43], s57 offen lds\n"
      "s_add_u32 m0, s60, 66560\n"
      "s_add_u32 s57, s61, 128\n"
      "buffer_load_dwordx4 v250, s[40:43], s57 offen lds\n"
      "s_add_u32 m0, s60, 67584\n"
      "s_add_u32 s57, s61, 256\n"
      "buffer_load_dwordx4 v250, s[40:43], s57 offen lds\n"
      "s_add_u32 m0, s60, 68608\n"
      "s_add_u32 s57, s61, 384\n"
      "buffer_load_dwordx4 v250, s[40:43], s57 offen lds\n"
      "s_mov_b64 s[36:37], %[pv]\n"
      "s_and_b32 s37, s37, 0xffff\n"
      "s_mov_b32 s38, 524288\n"
      "s_mov_b32 s39, 0x00020000\n"
      "s_mov_b64 s[44:45], %[pwo]\n"
      "s_and_b32 s45, s45, 0xffff\n"
      "s_mov_b32 s46, 229376\n"
      "s_mov_b32 s47, 0x00020000\n"
      "v_bfe_u32 v241, v251, 4, 1\n"
      "v_lshlrev_b32 v241, 1, v241\n"
      "v_xor_b32 v241, v241, v254\n"
      "v_lshlrev_b32 v241, 4, v241\n"
      "v_lshrrev_b32 v255, 3, v251\n"
      "v_mul_u32_u24 v255, 512, v255\n"
      "v_add_u32 v241, v255, v241\n"
      "v_xor_b32 v242, 64, v241\n"
      "s_lshl_b32 s55, s52, 13\n"
      "s_add_u32 s55, s55, 65536\n"
      "s_lshl_b32 s58, s52, 7\n"
      "s_add_u32 s94, %[sub], 0\n"
      "s_and_b32 s94, s94, 3\n"
      "s_lshl_b32 s94, s94, 14\n"
      "s_add_u32 s94, s94, s58\n"
      "s_add_u32 s95, %[sub], 1\n"
      "s_and_b32 s95, s95, 3\n"
      "s_lshl_b32 s95, s95, 14\n"
      "s_add_u32 s95, s95, s58\n"
      "s_add_u32 s96, %[sub], 2\n"
      "s_and_b32 s96, s96, 3\n"
      "s_lshl_b32 s96, s96, 14\n"
      "s_add_u32 s96, s96, s58\n"
      "s_add_u32 s97, %[sub], 3\n"
      "s_and_b32 s97, s97, 3\n"
      "s_lshl_b32 s97, s97, 14\n"
      "s_add_u32 s97, s97, s58\n"
      "v_bfe_u32 v244, v251, 3, 1\n"
      "v_and_b32 v255, 1, v252\n"
      "v_lshl_or_b32 v244, v255, 1, v244\n"
      "v_bfe_u32 v255, v251, 1, 1\n"
      "v_lshl_or_b32 v244, v244, 1, v255\n"
      "v_lshlrev_b32 v244, 4, v244\n"
      "v_and_b32 v255, 1, v251\n"
      "v_lshl_or_b32 v244, v255, 3, v244\n"
      "v_bfe_u32 v255, v251, 2, 2\n"
      "v_lshl_or_b32 v244, v255, 7, v244\n"
      "v_mul_u32_u24 v255, 1024, v252\n"
      "v_add_u32 v244, v255, v244\n"
      "v_add_u32 v244, s55, v244\n"
      "v_xor_b32 v245, 32, v244\n"
      "v_xor_b32 v246, 64, v244\n"
      "v_xor_b32 v247, 96, v244\n"
      "v_xor_b32 v248, v252, v254\n"
      "v_lshlrev_b32 v248, 4, v248\n"
      "v_lshl_add_u32 v248, v253, 7, v248\n"
      "v_add_u32 v248, 131072, v248\n"
      "v_xor_b32 v249, 64, v248\n"
      "v_mov_b32 v0, 0\n"
      "v_mov_b32 v1, 0\n"
      "v_mov_b32 v2, 0\n"
      "v_mov_b32 v3, 0\n"
      "v_mov_b32 v4, 0\n"
      "v_mov_b32 v5, 0\n"
      "v_mov_b32 v6, 0\n"
      "v_mov_b32 v7, 0\n"
      "v_mov_b32 v8, 0\n"
      "v_mov_b32 v9, 0\n"
      "v_mov_b32 v10, 0\n"
      "v_mov_b32 v11, 0\n"
      "v_mov_b32 v12, 0\n"
      "v_mov_b32 v13, 0\n"
      "v_mov_b32 v14, 0\n"
      "v_mov_b32 v15, 0\n"
      "v_mov_b32 v16, 0\n"
      "v_mov_b32 v17, 0\n"
      "v_mov_b32 v18, 0\n"
      "v_mov_b32 v19, 0\n"
      "v_mov_b32 v20, 0\n"
      "v_mov_b32 v21, 0\n"
      "v_mov_b32 v22, 0\n"
      "v_mov_b32 v23, 0\n"
      "v_mov_b32 v24, 0\n"
      "v_mov_b32 v25, 0\n"
      "v_mov_b32 v26, 0\n"
      "v_mov_b32 v27, 0\n"
      "v_mov_b32 v28, 0\n"
      "v_mov_b32 v29, 0\n"
      "v_mov_b32 v30, 0\n"
      "v_mov_b32 v31, 0\n"
      "v_mov_b32 v32, 0\n"
      "v_mov_b32 v33, 0\n"
      "v_mov_b32 v34, 0\n"
      "v_mov_b32 v35, 0\n"
      "v_mov_b32 v36, 0\n"
      "v_mov_b32 v37, 0\n"
      "v_mov_b32 v38, 0\n"
      "v_mov_b32 v39, 0\n"
      "v_mov_b32 v40, 0\n"
      "v_mov_b32 v41, 0\n"
      "v_mov_b32 v42, 0\n"
      "v_mov_b32 v43, 0\n"
      "v_mov_b32 v44, 0\n"
      "v_mov_b32 v45, 0\n"
      "v_mov_b32 v46, 0\n"
      "v_mov_b32 v47, 0\n"
      "v_mov_b32 v48, 0\n"
      "v_mov_b32 v49, 0\n"
      "v_mov_b32 v50, 0\n"
      "v_mov_b32 v51, 0\n"
      "v_mov_b32 v52, 0\n"
      "v_mov_b32 v53, 0\n"
      "v_mov_b32 v54, 0\n"
      "v_mov_b32 v55, 0\n"
      "v_mov_b32 v56, 0\n"
      "v_mov_b32 v57, 0\n"
      "v_mov_b32 v58, 0\n"
      "v_mov_b32 v59, 0\n"
      "v_mov_b32 v60, 0\n"
      "v_mov_b32 v61, 0\n"
      "v_mov_b32 v62, 0\n"
      "v_mov_b32 v63, 0\n"
      "v_mov_b32 v64, 0\n"
      "v_mov_b32 v65, 0\n"
      "v_mov_b32 v66, 0\n"
      "v_mov_b32 v67, 0\n"
      "v_mov_b32 v68, 0\n"
      "v_mov_b32 v69, 0\n"
      "v_mov_b32 v70, 0\n"
      "v_mov_b32 v71, 0\n"
      "v_mov_b32 v72, 0\n"
      "v_mov_b32 v73, 0\n"
      "v_mov_b32 v74, 0\n"
      "v_mov_b32 v75, 0\n"
      "v_mov_b32 v76, 0\n"
      "v_mov_b32 v77, 0\n"
      "v_mov_b32 v78, 0\n"
      "v_mov_b32 v79, 0\n"
      "v_bfe_u32 v243, v251, 4, 1\n"
      "s_and_b32 s57, s52, 1\n"
      "s_lshl_b32 s57, s57, 1\n"
      "v_or_b32 v243, s57, v243\n"
      "v_lshlrev_b32 v243, 1, v243\n"
      "v_xor_b32 v243, v243, v254\n"
      "v_lshlrev_b32 v243, 4, v243\n"
      "v_lshrrev_b32 v255, 3, v251\n"
      "v_mul_u32_u24 v255, 896, v255\n"
      "v_add_u32 v243, v255, v243\n"
      "s_mul_i32 s65, s52, 7168\n"
      "s_mul_i32 s66, s52, 7168\n"
      "v_lshrrev_b32 v255, 3, v251\n"
      "v_xor_b32 v239, v255, v254\n"
      "v_lshlrev_b32 v239, 4, v239\n"
      "v_mul_u32_u24 v255, 512, v255\n"
      "v_add_u32 v239, v255, v239\n"
      "s_waitcnt vmcnt(10)\n"
      "s_waitcnt lgkmcnt(0)\n"
      "s_barrier\n"
      "s_lshl_b32 s61, s93, 19\n"
      "s_add_u32 s61, s61, s59\n"
      "s_add_u32 m0, s60, 81920\n"
      "s_add_u32 s57, s61, 262144\n"
      "buffer_load_dwordx4 v250, s[40:43], s57 offen lds\n"
      "s_add_u32 m0, s60, 82944\n"
      "s_add_u32 s57, s61, 262272\n"
      "buffer_load_dwordx4 v250, s[40:43], s57 offen lds\n"
      "s_add_u32 m0, s60, 83968\n"
      "s_add_u32 s57, s61, 262400\n"
      "buffer_load_dwordx4 v250, s[40:43], s57 offen lds\n"
      "s_add_u32 m0, s60, 84992\n"
      "s_add_u32 s57, s61, 262528\n"
      "buffer_load_dwordx4 v250, s[40:43], s57 offen lds\n"
      "s_waitcnt vmcnt(4)\n"
      "s_waitcnt lgkmcnt(0)\n"
      "s_barrier\n"
      "s_mov_b64 s[48:49], %[pk]\n"
      "s_and_b32 s49, s49, 0xffff\n"
      "s_mov_b32 s50, 524288\n"
      "s_mov_b32 s51, 0x00020000\n"
      "s_lshr_b32 s57, s52, 1\n"
      "s_add_u32 s57, s57, %[sub]\n"
      "s_add_u32 s57, s57, 0\n"
      "s_and_b32 s57, s57, 3\n"
      "s_lshl_b32 s57, s57, 14\n"
      "s_and_b32 s58, s52, 1\n"
      "s_lshl_b32 s58, s58, 13\n"
      "s_add_u32 s59, s57, s58\n"
      "s_lshl_b32 s58, s52, 13\n"
      "s_add_u32 m0, s58, 0\n"
      "s_add_u32 s57, s59, 0\n"
      "buffer_load_dwordx4 v239, s[48:51], s57 offen nt lds\n"
      "s_add_u32 m0, s58, 1024\n"
      "s_add_u32 s57, s59, 128\n"
      "buffer_load_dwordx4 v239, s[48:51], s57 offen nt lds\n"
      "s_add_u32 m0, s58, 2048\n"
      "s_add_u32 s57, s59, 256\n"
      "buffer_load_dwordx4 v239, s[48:51], s57 offen nt lds\n"
      "s_add_u32 m0, s58, 3072\n"
      "s_add_u32 s57, s59, 384\n"
      "buffer_load_dwordx4 v239, s[48:51], s57 offen nt lds\n"
      "s_add_u32 m0, s58, 4096\n"
      "s_add_u32 s57, s59, 4096\n"
      "buffer_load_dwordx4 v239, s[48:51], s57 offen nt lds\n"
      "s_add_u32 m0, s58, 5120\n"
      "s_add_u32 s57, s59, 4224\n"
      "buffer_load_dwordx4 v239, s[48:51], s57 offen nt lds\n"
      "s_add_u32 m0, s58, 6144\n"
      "s_add_u32 s57, s59, 4352\n"
      "buffer_load_dwordx4 v239, s[48:51], s57 offen nt lds\n"
      "s_add_u32 m0, s58, 7168\n"
      "s_add_u32 s57, s59, 4480\n"
      "buffer_load_dwordx4 v239, s[48:51], s57 offen nt lds\n"
      "s_waitcnt vmcnt(8)\n"
      "s_waitcnt lgkmcnt(0)\n"
      "s_barrier\n"
      "s_lshr_b32 s57, s52, 1\n"
      "s_add_u32 s57, s57, %[sub]\n"
      "s_add_u32 s57, s57, 2\n"
      "s_and_b32 s57, s57, 3\n"
      "s_lshl_b32 s57, s57, 14\n"
      "s_and_b32 s58, s52, 1\n"
      "s_lshl_b32 s58, s58, 13\n"
      "s_add_u32 s59, s57, s58\n"
      "s_lshl_b32 s58, s52, 13\n"
      "s_add_u32 m0, s58, 32768\n"
      "s_add_u32 s57, s59, 0\n"
      "buffer_load_dwordx4 v239, s[48:51], s57 offen nt lds\n"
      "s_add_u32 m0, s58, 33792\n"
      "s_add_u32 s57, s59, 128\n"
      "buffer_load_dwordx4 v239, s[48:51], s57 offen nt lds\n"
      "s_add_u32 m0, s58, 34816\n"
      "s_add_u32 s57, s59, 256\n"
      "buffer_load_dwordx4 v239, s[48:51], s57 offen nt lds\n"
      "s_add_u32 m0, s58, 35840\n"
      "s_add_u32 s57, s59, 384\n"
      "buffer_load_dwordx4 v239, s[48:51], s57 offen nt lds\n"
      "s_add_u32 m0, s58, 36864\n"
      "s_add_u32 s57, s59, 4096\n"
      "buffer_load_dwordx4 v239, s[48:51], s57 offen nt lds\n"
      "s_add_u32 m0, s58, 37888\n"
      "s_add_u32 s57, s59, 4224\n"
      "buffer_load_dwordx4 v239, s[48:51], s57 offen nt lds\n"
      "s_add_u32 m0, s58, 38912\n"
      "s_add_u32 s57, s59, 4352\n"
      "buffer_load_dwordx4 v239, s[48:51], s57 offen nt lds\n"
      "s_add_u32 m0, s58, 39936\n"
      "s_add_u32 s57, s59, 4480\n"
      "buffer_load_dwordx4 v239, s[48:51], s57 offen nt lds\n"
      "s_waitcnt lgkmcnt(0)\n"
      "s_barrier\n"
      "s_add_u32 m0, s55, 0\n"
      "s_add_u32 s57, s94, 0\n"
      "buffer_load_dwordx4 v241, s[36:39], s57 offen nt lds\n"
      "s_add_u32 m0, s55, 1024\n"
      "s_add_u32 s57, s94, 4096\n"
      "buffer_load_dwordx4 v242, s[36:39], s57 offen nt lds\n"
      "s_add_u32 m0, s55, 2048\n"
      "s_add_u32 s57, s94, 8192\n"
      "buffer_load_dwordx4 v241, s[36:39], s57 offen nt lds\n"
      "s_add_u32 m0, s55, 3072\n"
      "s_add_u32 s57, s94, 12288\n"
      "buffer_load_dwordx4 v242, s[36:39], s57 offen nt lds\n"
      "s_add_u32 s94, s94, 65536\n"
      "s_add_u32 m0, s55, 4096\n"
      "s_add_u32 s57, s95, 0\n"
      "buffer_load_dwordx4 v241, s[36:39], s57 offen nt lds\n"
      "s_add_u32 m0, s55, 5120\n"
      "s_add_u32 s57, s95, 4096\n"
      "buffer_load_dwordx4 v242, s[36:39], s57 offen nt lds\n"
      "s_add_u32 m0, s55, 6144\n"
      "s_add_u32 s57, s95, 8192\n"
      "buffer_load_dwordx4 v241, s[36:39], s57 offen nt lds\n"
      "s_add_u32 m0, s55, 7168\n"
      "s_add_u32 s57, s95, 12288\n"
      "buffer_load_dwordx4 v242, s[36:39], s57 offen nt lds\n"
      "s_add_u32 s95, s95, 65536\n"
      "s_waitcnt vmcnt(16)\n"
      "s_waitcnt lgkmcnt(0)\n"
      "s_barrier\n"
      "s_cmp_lt_u32 s52, 2\n"
      "s_cbranch_scc1 LTW8%=\n"
      "v_lshlrev_b32 v232, 7, v251\n"
      "s_mul_i32 s57, %[br], 14336\n"
      "s_and_b32 s58, s52, 1\n"
      "s_lshl_b32 s58, s58, 13\n"
      "s_add_u32 s57, s57, s58\n"
      "buffer_load_dword v233, v232, s[44:47], s57 offen\n"
      "LTW8%=:\n"
      "s_waitcnt vmcnt(0)\n"
      "ds_read_b64_tr_b16 v[80:81], v244 offset:0\n"
      "ds_read_b64_tr_b16 v[82:83], v244 offset:512\n"
      "ds_read_b64_tr_b16 v[84:85], v245 offset:0\n"
      "ds_read_b64_tr_b16 v[86:87], v245 offset:512\n"
      "ds_read_b64_tr_b16 v[88:89], v246 offset:0\n"
      "ds_read_b64_tr_b16 v[90:91], v246 offset:512\n"
      "ds_read_b64_tr_b16 v[92:93], v247 offset:0\n"
      "ds_read_b64_tr_b16 v[94:95], v247 offset:512\n"
      "ds_read_b64_tr_b16 v[96:97], v244 offset:4096\n"
      "ds_read_b64_tr_b16 v[98:99], v244 offset:4608\n"
      "ds_read_b64_tr_b16 v[100:101], v245 offset:4096\n"
      "ds_read_b64_tr_b16 v[102:103], v245 offset:4608\n"
      "ds_read_b64_tr_b16 v[104:105], v246 offset:4096\n"
      "ds_read_b64_tr_b16 v[106:107], v246 offset:4608\n"
      "ds_read_b64_tr_b16 v[108:109], v247 offset:4096\n"
      "ds_read_b64_tr_b16 v[110:111], v247 offset:4608\n"
      "s_waitcnt lgkmcnt(0)\n"
      "s_barrier\n"
      "s_add_u32 m0, s55, 32768\n"
      "s_add_u32 s57, s96, 0\n"
      "buffer_load_dwordx4 v241, s[36:39], s57 offen nt lds\n"
      "s_add_u32 m0, s55, 33792\n"
      "s_add_u32 s57, s96, 4096\n"
      "buffer_load_dwordx4 v242, s[36:39], s57 offen nt lds\n"
      "s_add_u32 m0, s55, 34816\n"
      "s_add_u32 s57, s96, 8192\n"
      "buffer_load_dwordx4 v241, s[36:39], s57 offen nt lds\n"
      "s_add_u32 m0, s55, 35840\n"
      "s_add_u32 s57, s96, 12288\n"
      "buffer_load_dwordx4 v242, s[36:39], s57 offen nt lds\n"
      "s_add_u32 s96, s96, 65536\n"
      "s_add_u32 m0, s55, 36864\n"
      "s_add_u32 s57, s97, 0\n"
      "buffer_load_dwordx4 v241, s[36:39], s57 offen nt lds\n"
      "s_add_u32 m0, s55, 37888\n"
      "s_add_u32 s57, s97, 4096\n"
      "buffer_load_dwordx4 v242, s[36:39], s57 offen nt lds\n"
      "s_add_u32 m0, s55, 38912\n"
      "s_add_u32 s57, s97, 8192\n"
      "buffer_load_dwordx4 v241, s[36:39], s57 offen nt lds\n"
      "s_add_u32 m0, s55, 39936\n"
      "s_add_u32 s57, s97, 12288\n"
      "buffer_load_dwordx4 v242, s[36:39], s57 offen nt lds\n"
      "s_add_u32 s97, s97, 65536\n"
      "s_waitcnt lgkmcnt(0)\n"
      "s_barrier\n"
      "s_add_u32 m0, s55, 0\n"
      "s_add_u32 s57, s94, 0\n"
      "buffer_load_dwordx4 v241, s[36:39], s57 offen nt lds\n"
      "s_add_u32 m0, s55, 1024\n"
      "s_add_u32 s57, s94, 4096\n"
      "buffer_load_dwordx4 v242, s[36:39], s57 offen nt lds\n"
      "s_add_u32 m0, s55, 2048\n"
      "s_add_u32 s57, s94, 8192\n"
      "buffer_load_dwordx4 v241, s[36:39], s57 offen nt lds\n"
      "s_add_u32 m0, s55, 3072\n"
      "s_add_u32 s57, s94, 12288\n"
      "buffer_load_dwordx4 v242, s[36:39], s57 offen nt lds\n"
      "s_add_u32 s94, s94, 65536\n"
      "s_add_u32 m0, s55, 4096\n"
      "s_add_u32 s57, s95, 0\n"
      "buffer_load_dwordx4 v241, s[36:39], s57 offen nt lds\n"
      "s_add_u32 m0, s55, 5120\n"
      "s_add_u32 s57, s95, 4096\n"
      "buffer_load_dwordx4 v242, s[36:39], s57 offen nt lds\n"
      "s_add_u32 m0, s55, 6144\n"
      "s_add_u32 s57, s95, 8192\n"
      "buffer_load_dwordx4 v241, s[36:39], s57 offen nt lds\n"
      "s_add_u32 m0, s55, 7168\n"
      "s_add_u32 s57, s95, 12288\n"
      "buffer_load_dwordx4 v242, s[36:39], s57 offen nt lds\n"
      "s_add_u32 s95, s95, 65536\n"
      "s_mov_b32 s54, 0\n"
      "LPV%=:\n"
      "ds_read_b128 v[144:147], v248 offset:0\n"
      "ds_read_b128 v[148:151], v248 offset:2048\n"
      "ds_read_b128 v[152:155], v248 offset:4096\n"
      "ds_read_b128 v[156:159], v248 offset:6144\n"
      "ds_read_b128 v[160:163], v248 offset:8192\n"
      "ds_read_b128 v[164:167], v249 offset:0\n"
      "ds_read_b128 v[168:171], v249 offset:2048\n"
      "ds_read_b128 v[172:175], v249 offset:4096\n"
      "ds_read_b128 v[176:179], v249 offset:6144\n"
      "ds_read_b128 v[180:183], v249 offset:8192\n"
      "s_waitcnt lgkmcnt(9)\n"
      "v_mfma_f32_16x16x32_bf16 v[0:3], v[80:83], v[144:147], v[0:3]\n"
      "s_waitcnt lgkmcnt(8)\n"
      "v_mfma_f32_16x16x32_bf16 v[4:7], v[80:83], v[148:151], v[4:7]\n"
      "s_waitcnt lgkmcnt(7)\n"
      "v_mfma_f32_16x16x32_bf16 v[8:11], v[80:83], v[152:155], v[8:11]\n"
      "s_waitcnt lgkmcnt(6)\n"
      "v_mfma_f32_16x16x32_bf16 v[12:15], v[80:83], v[156:159], v[12:15]\n"
      "s_waitcnt lgkmcnt(5)\n"
      "v_mfma_f32_16x16x32_bf16 v[16:19], v[80:83], v[160:163], v[16:19]\n"
      "v_mfma_f32_16x16x32_bf16 v[20:23], v[84:87], v[144:147], v[20:23]\n"
      "s_waitcnt vmcnt(8)\n"
      "ds_read_b64_tr_b16 v[112:113], v244 offset:32768\n"
      "v_mfma_f32_16x16x32_bf16 v[24:27], v[84:87], v[148:151], v[24:27]\n"
      "ds_read_b64_tr_b16 v[114:115], v244 offset:33280\n"
      "v_mfma_f32_16x16x32_bf16 v[28:31], v[84:87], v[152:155], v[28:31]\n"
      "ds_read_b64_tr_b16 v[116:117], v245 offset:32768\n"
      "v_mfma_f32_16x16x32_bf16 v[32:35], v[84:87], v[156:159], v[32:35]\n"
      "ds_read_b64_tr_b16 v[118:119], v245 offset:33280\n"
      "v_mfma_f32_16x16x32_bf16 v[36:39], v[84:87], v[160:163], v[36:39]\n"
      "ds_read_b64_tr_b16 v[120:121], v246 offset:32768\n"
      "v_mfma_f32_16x16x32_bf16 v[40:43], v[88:91], v[144:147], v[40:43]\n"
      "ds_read_b64_tr_b16 v[122:123], v246 offset:33280\n"
      "v_mfma_f32_16x16x32_bf16 v[44:47], v[88:91], v[148:151], v[44:47]\n"
      "ds_read_b64_tr_b16 v[124:125], v247 offset:32768\n"
      "v_mfma_f32_16x16x32_bf16 v[48:51], v[88:91], v[152:155], v[48:51]\n"
      "ds_read_b64_tr_b16 v[126:127], v247 offset:33280\n"
      "ds_read_b64_tr_b16 v[128:129], v244 offset:36864\n"
      "v_mfma_f32_16x16x32_bf16 v[52:55], v[88:91], v[156:159], v[52:55]\n"
      "ds_read_b64_tr_b16 v[130:131], v244 offset:37376\n"
      "v_mfma_f32_16x16x32_bf16 v[56:59], v[88:91], v[160:163], v[56:59]\n"
      "ds_read_b64_tr_b16 v[132:133], v245 offset:36864\n"
      "v_mfma_f32_16x16x32_bf16 v[60:63], v[92:95], v[144:147], v[60:63]\n"
      "ds_read_b64_tr_b16 v[134:135], v245 offset:37376\n"
      "v_mfma_f32_16x16x32_bf16 v[64:67], v[92:95], v[148:151], v[64:67]\n"
      "ds_read_b64_tr_b16 v[136:137], v246 offset:36864\n"
      "v_mfma_f32_16x16x32_bf16 v[68:71], v[92:95], v[152:155], v[68:71]\n"
      "ds_read_b64_tr_b16 v[138:139], v246 offset:37376\n"
      "v_mfma_f32_16x16x32_bf16 v[72:75], v[92:95], v[156:159], v[72:75]\n"
      "ds_read_b64_tr_b16 v[140:141], v247 offset:36864\n"
      "v_mfma_f32_16x16x32_bf16 v[76:79], v[92:95], v[160:163], v[76:79]\n"
      "ds_read_b64_tr_b16 v[142:143], v247 offset:37376\n"
      "s_waitcnt lgkmcnt(15)\n"
      "v_mfma_f32_16x16x32_bf16 v[0:3], v[96:99], v[164:167], v[0:3]\n"
      "v_mfma_f32_16x16x32_bf16 v[4:7], v[96:99], v[168:171], v[4:7]\n"
      "v_mfma_f32_16x16x32_bf16 v[8:11], v[96:99], v[172:175], v[8:11]\n"
      "v_mfma_f32_16x16x32_bf16 v[12:15], v[96:99], v[176:179], v[12:15]\n"
      "s_waitcnt lgkmcnt(0)\n"
      "v_mfma_f32_16x16x32_bf16 v[16:19], v[96:99], v[180:183], v[16:19]\n"
      "s_add_u32 m0, s55, 32768\n"
      "s_add_u32 s57, s96, 0\n"
      "buffer_load_dwordx4 v241, s[36:39], s57 offen nt lds\n"
      "v_mfma_f32_16x16x32_bf16 v[20:23], v[100:103], v[164:167], v[20:23]\n"
      "v_mfma_f32_16x16x32_bf16 v[24:27], v[100:103], v[168:171], v[24:27]\n"
      "s_add_u32 m0, s55, 33792\n"
      "s_add_u32 s57, s96, 4096\n"
      "buffer_load_dwordx4 v242, s[36:39], s57 offen nt lds\n"
      "v_mfma_f32_16x16x32_bf16 v[28:31], v[100:103], v[172:175], v[28:31]\n"
      "v_mfma_f32_16x16x32_bf16 v[32:35], v[100:103], v[176:179], v[32:35]\n"
      "s_add_u32 m0, s55, 34816\n"
      "s_add_u32 s57, s96, 8192\n"
      "buffer_load_dwordx4 v241, s[36:39], s57 offen nt lds\n"
      "v_mfma_f32_16x16x32_bf16 v[36:39], v[100:103], v[180:183], v[36:39]\n"
      "v_mfma_f32_16x16x32_bf16 v[40:43], v[104:107], v[164:167], v[40:43]\n"
      "s_add_u32 m0, s55, 35840\n"
      "s_add_u32 s57, s96, 12288\n"
      "buffer_load_dwordx4 v242, s[36:39], s57 offen nt lds\n"
      "s_add_u32 s96, s96, 65536\n"
      "v_mfma_f32_16x16x32_bf16 v[44:47], v[104:107], v[168:171], v[44:47]\n"
      "v_mfma_f32_16x16x32_bf16 v[48:51], v[104:107], v[172:175], v[48:51]\n"
      "s_add_u32 m0, s55, 36864\n"
      "s_add_u32 s57, s97, 0\n"
      "buffer_load_dwordx4 v241, s[36:39], s57 offen nt lds\n"
      "v_mfma_f32_16x16x32_bf16 v[52:55], v[104:107], v[176:179], v[52:55]\n"
      "v_mfma_f32_16x16x32_bf16 v[56:59], v[104:107], v[180:183], v[56:59]\n"
      "s_add_u32 m0, s55, 37888\n"
      "s_add_u32 s57, s97, 4096\n"
      "buffer_load_dwordx4 v242, s[36:39], s57 offen nt lds\n"
      "v_mfma_f32_16x16x32_bf16 v[60:63], v[108:111], v[164:167], v[60:63]\n"
      "v_mfma_f32_16x16x32_bf16 v[64:67], v[108:111], v[168:171], v[64:67]\n"
      "s_add_u32 m0, s55, 38912\n"
      "s_add_u32 s57, s97, 8192\n"
      "buffer_load_dwordx4 v241, s[36:39], s57 offen nt lds\n"
      "v_mfma_f32_16x16x32_bf16 v[68:71], v[108:111], v[172:175], v[68:71]\n"
      "v_mfma_f32_16x16x32_bf16 v[72:75], v[108:111], v[176:179], v[72:75]\n"
      "s_add_u32 m0, s55, 39936\n"
      "s_add_u32 s57, s97, 12288\n"
      "buffer_load_dwordx4 v242, s[36:39], s57 offen nt lds\n"
      "s_add_u32 s97, s97, 65536\n"
      "v_mfma_f32_16x16x32_bf16 v[76:79], v[108:111], v[180:183], v[76:79]\n"
      "s_waitcnt lgkmcnt(0)\n"
      "s_barrier\n"
      "ds_read_b128 v[144:147], v248 offset:10240\n"
      "ds_read_b128 v[148:151], v248 offset:12288\n"
      "ds_read_b128 v[152:155], v248 offset:14336\n"
      "ds_read_b128 v[156:159], v248 offset:16384\n"
      "ds_read_b128 v[160:163], v248 offset:18432\n"
      "ds_read_b128 v[164:167], v249 offset:10240\n"
      "ds_read_b128 v[168:171], v249 offset:12288\n"
      "ds_read_b128 v[172:175], v249 offset:14336\n"
      "ds_read_b128 v[176:179], v249 offset:16384\n"
      "ds_read_b128 v[180:183], v249 offset:18432\n"
      "s_waitcnt lgkmcnt(9)\n"
      "v_mfma_f32_16x16x32_bf16 v[0:3], v[112:115], v[144:147], v[0:3]\n"
      "s_waitcnt lgkmcnt(8)\n"
      "v_mfma_f32_16x16x32_bf16 v[4:7], v[112:115], v[148:151], v[4:7]\n"
      "s_waitcnt lgkmcnt(7)\n"
      "v_mfma_f32_16x16x32_bf16 v[8:11], v[112:115], v[152:155], v[8:11]\n"
      "s_waitcnt lgkmcnt(6)\n"
      "v_mfma_f32_16x16x32_bf16 v[12:15], v[112:115], v[156:159], v[12:15]\n"
      "s_waitcnt lgkmcnt(5)\n"
      "v_mfma_f32_16x16x32_bf16 v[16:19], v[112:115], v[160:163], v[16:19]\n"
      "v_mfma_f32_16x16x32_bf16 v[20:23], v[116:119], v[144:147], v[20:23]\n"
      "s_waitcnt vmcnt(8)\n"
      "ds_read_b64_tr_b16 v[80:81], v244 offset:0\n"
      "v_mfma_f32_16x16x32_bf16 v[24:27], v[116:119], v[148:151], v[24:27]\n"
      "ds_read_b64_tr_b16 v[82:83], v244 offset:512\n"
      "v_mfma_f32_16x16x32_bf16 v[28:31], v[116:119], v[152:155], v[28:31]\n"
      "ds_read_b64_tr_b16 v[84:85], v245 offset:0\n"
      "v_mfma_f32_16x16x32_bf16 v[32:35], v[116:119], v[156:159], v[32:35]\n"
      "ds_read_b64_tr_b16 v[86:87], v245 offset:512\n"
      "v_mfma_f32_16x16x32_bf16 v[36:39], v[116:119], v[160:163], v[36:39]\n"
      "ds_read_b64_tr_b16 v[88:89], v246 offset:0\n"
      "v_mfma_f32_16x16x32_bf16 v[40:43], v[120:123], v[144:147], v[40:43]\n"
      "ds_read_b64_tr_b16 v[90:91], v246 offset:512\n"
      "v_mfma_f32_16x16x32_bf16 v[44:47], v[120:123], v[148:151], v[44:47]\n"
      "ds_read_b64_tr_b16 v[92:93], v247 offset:0\n"
      "v_mfma_f32_16x16x32_bf16 v[48:51], v[120:123], v[152:155], v[48:51]\n"
      "ds_read_b64_tr_b16 v[94:95], v247 offset:512\n"
      "ds_read_b64_tr_b16 v[96:97], v244 offset:4096\n"
      "v_mfma_f32_16x16x32_bf16 v[52:55], v[120:123], v[156:159], v[52:55]\n"
      "ds_read_b64_tr_b16 v[98:99], v244 offset:4608\n"
      "v_mfma_f32_16x16x32_bf16 v[56:59], v[120:123], v[160:163], v[56:59]\n"
      "ds_read_b64_tr_b16 v[100:101], v245 offset:4096\n"
      "v_mfma_f32_16x16x32_bf16 v[60:63], v[124:127], v[144:147], v[60:63]\n"
      "ds_read_b64_tr_b16 v[102:103], v245 offset:4608\n"
      "v_mfma_f32_16x16x32_bf16 v[64:67], v[124:127], v[148:151], v[64:67]\n"
      "ds_read_b64_tr_b16 v[104:105], v246 offset:4096\n"
      "v_mfma_f32_16x16x32_bf16 v[68:71], v[124:127], v[152:155], v[68:71]\n"
      "ds_read_b64_tr_b16 v[106:107], v246 offset:4608\n"
      "v_mfma_f32_16x16x32_bf16 v[72:75], v[124:127], v[156:159], v[72:75]\n"
      "ds_read_b64_tr_b16 v[108:109], v247 offset:4096\n"
      "v_mfma_f32_16x16x32_bf16 v[76:79], v[124:127], v[160:163], v[76:79]\n"
      "ds_read_b64_tr_b16 v[110:111], v247 offset:4608\n"
      "s_waitcnt lgkmcnt(15)\n"
      "v_mfma_f32_16x16x32_bf16 v[0:3], v[128:131], v[164:167], v[0:3]\n"
      "v_mfma_f32_16x16x32_bf16 v[4:7], v[128:131], v[168:171], v[4:7]\n"
      "v_mfma_f32_16x16x32_bf16 v[8:11], v[128:131], v[172:175], v[8:11]\n"
      "v_mfma_f32_16x16x32_bf16 v[12:15], v[128:131], v[176:179], v[12:15]\n"
      "s_waitcnt lgkmcnt(0)\n"
      "v_mfma_f32_16x16x32_bf16 v[16:19], v[128:131], v[180:183], v[16:19]\n"
      "s_add_u32 m0, s55, 0\n"
      "s_add_u32 s57, s94, 0\n"
      "buffer_load_dwordx4 v241, s[36:39], s57 offen nt lds\n"
      "v_mfma_f32_16x16x32_bf16 v[20:23], v[132:135], v[164:167], v[20:23]\n"
      "v_mfma_f32_16x16x32_bf16 v[24:27], v[132:135], v[168:171], v[24:27]\n"
      "s_add_u32 m0, s55, 1024\n"
      "s_add_u32 s57, s94, 4096\n"
      "buffer_load_dwordx4 v242, s[36:39], s57 offen nt lds\n"
      "v_mfma_f32_16x16x32_bf16 v[28:31], v[132:135], v[172:175], v[28:31]\n"
      "v_mfma_f32_16x16x32_bf16 v[32:35], v[132:135], v[176:179], v[32:35]\n"
      "s_add_u32 m0, s55, 2048\n"
      "s_add_u32 s57, s94, 8192\n"
      "buffer_load_dwordx4 v241, s[36:39], s57 offen nt lds\n"
      "v_mfma_f32_16x16x32_bf16 v[36:39], v[132:135], v[180:183], v[36:39]\n"
      "v_mfma_f32_16x16x32_bf16 v[40:43], v[136:139], v[164:167], v[40:43]\n"
      "s_add_u32 m0, s55, 3072\n"
      "s_add_u32 s57, s94, 12288\n"
      "buffer_load_dwordx4 v242, s[36:39], s57 offen nt lds\n"
      "s_add_u32 s94, s94, 65536\n"
      "v_mfma_f32_16x16x32_bf16 v[44:47], v[136:139], v[168:171], v[44:47]\n"
      "v_mfma_f32_16x16x32_bf16 v[48:51], v[136:139], v[172:175], v[48:51]\n"
      "s_add_u32 m0, s55, 4096\n"
      "s_add_u32 s57, s95, 0\n"
      "buffer_load_dwordx4 v241, s[36:39], s57 offen nt lds\n"
      "v_mfma_f32_16x16x32_bf16 v[52:55], v[136:139], v[176:179], v[52:55]\n"
      "v_mfma_f32_16x16x32_bf16 v[56:59], v[136:139], v[180:183], v[56:59]\n"
      "s_add_u32 m0, s55, 5120\n"
      "s_add_u32 s57, s95, 4096\n"
      "buffer_load_dwordx4 v242, s[36:39], s57 offen nt lds\n"
      "v_mfma_f32_16x16x32_bf16 v[60:63], v[140:143], v[164:167], v[60:63]\n"
      "v_mfma_f32_16x16x32_bf16 v[64:67], v[140:143], v[168:171], v[64:67]\n"
      "s_add_u32 m0, s55, 6144\n"
      "s_add_u32 s57, s95, 8192\n"
      "buffer_load_dwordx4 v241, s[36:39], s57 offen nt lds\n"
      "v_mfma_f32_16x16x32_bf16 v[68:71], v[140:143], v[172:175], v[68:71]\n"
      "v_mfma_f32_16x16x32_bf16 v[72:75], v[140:143], v[176:179], v[72:75]\n"
      "s_add_u32 m0, s55, 7168\n"
      "s_add_u32 s57, s95, 12288\n"
      "buffer_load_dwordx4 v242, s[36:39], s57 offen nt lds\n"
      "s_add_u32 s95, s95, 65536\n"
      "v_mfma_f32_16x16x32_bf16 v[76:79], v[140:143], v[180:183], v[76:79]\n"
      "s_waitcnt lgkmcnt(0)\n"
      "s_barrier\n"
      "s_add_u32 s54, s54, 1\n"
      "s_cmp_lt_u32 s54, 6\n"
      "s_cbranch_scc1 LPV%=\n"
      "ds_read_b128 v[144:147], v248 offset:0\n"
      "ds_read_b128 v[148:151], v248 offset:2048\n"
      "ds_read_b128 v[152:155], v248 offset:4096\n"
      "ds_read_b128 v[156:159], v248 offset:6144\n"
      "ds_read_b128 v[160:163], v248 offset:8192\n"
      "ds_read_b128 v[164:167], v249 offset:0\n"
      "ds_read_b128 v[168:171], v249 offset:2048\n"
      "ds_read_b128 v[172:175], v249 offset:4096\n"
      "ds_read_b128 v[176:179], v249 offset:6144\n"
      "ds_read_b128 v[180:183], v249 offset:8192\n"
      "s_waitcnt lgkmcnt(9)\n"
      "v_mfma_f32_16x16x32_bf16 v[0:3], v[80:83], v[144:147], v[0:3]\n"
      "s_waitcnt lgkmcnt(8)\n"
      "v_mfma_f32_16x16x32_bf16 v[4:7], v[80:83], v[148:151], v[4:7]\n"
      "s_waitcnt lgkmcnt(7)\n"
      "v_mfma_f32_16x16x32_bf16 v[8:11], v[80:83], v[152:155], v[8:11]\n"
      "s_waitcnt lgkmcnt(6)\n"
      "v_mfma_f32_16x16x32_bf16 v[12:15], v[80:83], v[156:159], v[12:15]\n"
      "s_waitcnt lgkmcnt(5)\n"
      "v_mfma_f32_16x16x32_bf16 v[16:19], v[80:83], v[160:163], v[16:19]\n"
      "v_mfma_f32_16x16x32_bf16 v[20:23], v[84:87], v[144:147], v[20:23]\n"
      "s_waitcnt vmcnt(8)\n"
      "ds_read_b64_tr_b16 v[112:113], v244 offset:32768\n"
      "v_mfma_f32_16x16x32_bf16 v[24:27], v[84:87], v[148:151], v[24:27]\n"
      "ds_read_b64_tr_b16 v[114:115], v244 offset:33280\n"
      "v_mfma_f32_16x16x32_bf16 v[28:31], v[84:87], v[152:155], v[28:31]\n"
      "ds_read_b64_tr_b16 v[116:117], v245 offset:32768\n"
      "v_mfma_f32_16x16x32_bf16 v[32:35], v[84:87], v[156:159], v[32:35]\n"
      "ds_read_b64_tr_b16 v[118:119], v245 offset:33280\n"
      "v_mfma_f32_16x16x32_bf16 v[36:39], v[84:87], v[160:163], v[36:39]\n"
      "ds_read_b64_tr_b16 v[120:121], v246 offset:32768\n"
      "v_mfma_f32_16x16x32_bf16 v[40:43], v[88:91], v[144:147], v[40:43]\n"
      "ds_read_b64_tr_b16 v[122:123], v246 offset:33280\n"
      "v_mfma_f32_16x16x32_bf16 v[44:47], v[88:91], v[148:151], v[44:47]\n"
      "ds_read_b64_tr_b16 v[124:125], v247 offset:32768\n"
      "v_mfma_f32_16x16x32_bf16 v[48:51], v[88:91], v[152:155], v[48:51]\n"
      "ds_read_b64_tr_b16 v[126:127], v247 offset:33280\n"
      "ds_read_b64_tr_b16 v[128:129], v244 offset:36864\n"
      "v_mfma_f32_16x16x32_bf16 v[52:55], v[88:91], v[156:159], v[52:55]\n"
      "ds_read_b64_tr_b16 v[130:131], v244 offset:37376\n"
      "v_mfma_f32_16x16x32_bf16 v[56:59], v[88:91], v[160:163], v[56:59]\n"
      "ds_read_b64_tr_b16 v[132:133], v245 offset:36864\n"
      "v_mfma_f32_16x16x32_bf16 v[60:63], v[92:95], v[144:147], v[60:63]\n"
      "ds_read_b64_tr_b16 v[134:135], v245 offset:37376\n"
      "v_mfma_f32_16x16x32_bf16 v[64:67], v[92:95], v[148:151], v[64:67]\n"
      "ds_read_b64_tr_b16 v[136:137], v246 offset:36864\n"
      "v_mfma_f32_16x16x32_bf16 v[68:71], v[92:95], v[152:155], v[68:71]\n"
      "ds_read_b64_tr_b16 v[138:139], v246 offset:37376\n"
      "v_mfma_f32_16x16x32_bf16 v[72:75], v[92:95], v[156:159], v[72:75]\n"
      "ds_read_b64_tr_b16 v[140:141], v247 offset:36864\n"
      "v_mfma_f32_16x16x32_bf16 v[76:79], v[92:95], v[160:163], v[76:79]\n"
      "ds_read_b64_tr_b16 v[142:143], v247 offset:37376\n"
      "s_waitcnt lgkmcnt(15)\n"
      "v_mfma_f32_16x16x32_bf16 v[0:3], v[96:99], v[164:167], v[0:3]\n"
      "v_mfma_f32_16x16x32_bf16 v[4:7], v[96:99], v[168:171], v[4:7]\n"
      "v_mfma_f32_16x16x32_bf16 v[8:11], v[96:99], v[172:175], v[8:11]\n"
      "v_mfma_f32_16x16x32_bf16 v[12:15], v[96:99], v[176:179], v[12:15]\n"
      "s_waitcnt lgkmcnt(0)\n"
      "v_mfma_f32_16x16x32_bf16 v[16:19], v[96:99], v[180:183], v[16:19]\n"
      "s_add_u32 m0, s55, 32768\n"
      "s_add_u32 s57, s96, 0\n"
      "buffer_load_dwordx4 v241, s[36:39], s57 offen nt lds\n"
      "v_mfma_f32_16x16x32_bf16 v[20:23], v[100:103], v[164:167], v[20:23]\n"
      "v_mfma_f32_16x16x32_bf16 v[24:27], v[100:103], v[168:171], v[24:27]\n"
      "s_add_u32 m0, s55, 33792\n"
      "s_add_u32 s57, s96, 4096\n"
      "buffer_load_dwordx4 v242, s[36:39], s57 offen nt lds\n"
      "v_mfma_f32_16x16x32_bf16 v[28:31], v[100:103], v[172:175], v[28:31]\n"
      "v_mfma_f32_16x16x32_bf16 v[32:35], v[100:103], v[176:179], v[32:35]\n"
      "s_add_u32 m0, s55, 34816\n"
      "s_add_u32 s57, s96, 8192\n"
      "buffer_load_dwordx4 v241, s[36:39], s57 offen nt lds\n"
      "v_mfma_f32_16x16x32_bf16 v[36:39], v[100:103], v[180:183], v[36:39]\n"
      "v_mfma_f32_16x16x32_bf16 v[40:43], v[104:107], v[164:167], v[40:43]\n"
      "s_add_u32 m0, s55, 35840\n"
      "s_add_u32 s57, s96, 12288\n"
      "buffer_load_dwordx4 v242, s[36:39], s57 offen nt lds\n"
      "s_add_u32 s96, s96, 65536\n"
      "v_mfma_f32_16x16x32_bf16 v[44:47], v[104:107], v[168:171], v[44:47]\n"
      "v_mfma_f32_16x16x32_bf16 v[48:51], v[104:107], v[172:175], v[48:51]\n"
      "s_add_u32 m0, s55, 36864\n"
      "s_add_u32 s57, s97, 0\n"
      "buffer_load_dwordx4 v241, s[36:39], s57 offen nt lds\n"
      "v_mfma_f32_16x16x32_bf16 v[52:55], v[104:107], v[176:179], v[52:55]\n"
      "v_mfma_f32_16x16x32_bf16 v[56:59], v[104:107], v[180:183], v[56:59]\n"
      "s_add_u32 m0, s55, 37888\n"
      "s_add_u32 s57, s97, 4096\n"
      "buffer_load_dwordx4 v242, s[36:39], s57 offen nt lds\n"
      "v_mfma_f32_16x16x32_bf16 v[60:63], v[108:111], v[164:167], v[60:63]\n"
      "v_mfma_f32_16x16x32_bf16 v[64:67], v[108:111], v[168:171], v[64:67]\n"
      "s_add_u32 m0, s55, 38912\n"
      "s_add_u32 s57, s97, 8192\n"
      "buffer_load_dwordx4 v241, s[36:39], s57 offen nt lds\n"
      "v_mfma_f32_16x16x32_bf16 v[68:71], v[108:111], v[172:175], v[68:71]\n"
      "v_mfma_f32_16x16x32_bf16 v[72:75], v[108:111], v[176:179], v[72:75]\n"
      "s_add_u32 m0, s55, 39936\n"
      "s_add_u32 s57, s97, 12288\n"
      "buffer_load_dwordx4 v242, s[36:39], s57 offen nt lds\n"
      "s_add_u32 s97, s97, 65536\n"
      "v_mfma_f32_16x16x32_bf16 v[76:79], v[108:111], v[180:183], v[76:79]\n"
      "s_waitcnt lgkmcnt(0)\n"
      "s_barrier\n"
      "ds_read_b128 v[144:147], v248 offset:10240\n"
      "ds_read_b128 v[148:151], v248 offset:12288\n"
      "ds_read_b128 v[152:155], v248 offset:14336\n"
      "ds_read_b128 v[156:159], v248 offset:16384\n"
      "ds_read_b128 v[160:163], v248 offset:18432\n"
      "ds_read_b128 v[164:167], v249 offset:10240\n"
      "ds_read_b128 v[168:171], v249 offset:12288\n"
      "ds_read_b128 v[172:175], v249 offset:14336\n"
      "ds_read_b128 v[176:179], v249 offset:16384\n"
      "ds_read_b128 v[180:183], v249 offset:18432\n"
      "s_waitcnt lgkmcnt(9)\n"
      "v_mfma_f32_16x16x32_bf16 v[0:3], v[112:115], v[144:147], v[0:3]\n"
      "s_waitcnt lgkmcnt(8)\n"
      "v_mfma_f32_16x16x32_bf16 v[4:7], v[112:115], v[148:151], v[4:7]\n"
      "s_waitcnt lgkmcnt(7)\n"
      "v_mfma_f32_16x16x32_bf16 v[8:11], v[112:115], v[152:155], v[8:11]\n"
      "s_waitcnt lgkmcnt(6)\n"
      "v_mfma_f32_16x16x32_bf16 v[12:15], v[112:115], v[156:159], v[12:15]\n"
      "s_waitcnt lgkmcnt(5)\n"
      "v_mfma_f32_16x16x32_bf16 v[16:19], v[112:115], v[160:163], v[16:19]\n"
      "v_mfma_f32_16x16x32_bf16 v[20:23], v[116:119], v[144:147], v[20:23]\n"
      "s_waitcnt vmcnt(8)\n"
      "ds_read_b64_tr_b16 v[80:81], v244 offset:0\n"
      "v_mfma_f32_16x16x32_bf16 v[24:27], v[116:119], v[148:151], v[24:27]\n"
      "ds_read_b64_tr_b16 v[82:83], v244 offset:512\n"
      "v_mfma_f32_16x16x32_bf16 v[28:31], v[116:119], v[152:155], v[28:31]\n"
      "ds_read_b64_tr_b16 v[84:85], v245 offset:0\n"
      "v_mfma_f32_16x16x32_bf16 v[32:35], v[116:119], v[156:159], v[32:35]\n"
      "ds_read_b64_tr_b16 v[86:87], v245 offset:512\n"
      "v_mfma_f32_16x16x32_bf16 v[36:39], v[116:119], v[160:163], v[36:39]\n"
      "ds_read_b64_tr_b16 v[88:89], v246 offset:0\n"
      "v_mfma_f32_16x16x32_bf16 v[40:43], v[120:123], v[144:147], v[40:43]\n"
      "ds_read_b64_tr_b16 v[90:91], v246 offset:512\n"
      "v_mfma_f32_16x16x32_bf16 v[44:47], v[120:123], v[148:151], v[44:47]\n"
      "ds_read_b64_tr_b16 v[92:93], v247 offset:0\n"
      "v_mfma_f32_16x16x32_bf16 v[48:51], v[120:123], v[152:155], v[48:51]\n"
      "ds_read_b64_tr_b16 v[94:95], v247 offset:512\n"
      "ds_read_b64_tr_b16 v[96:97], v244 offset:4096\n"
      "v_mfma_f32_16x16x32_bf16 v[52:55], v[120:123], v[156:159], v[52:55]\n"
      "ds_read_b64_tr_b16 v[98:99], v244 offset:4608\n"
      "v_mfma_f32_16x16x32_bf16 v[56:59], v[120:123], v[160:163], v[56:59]\n"
      "ds_read_b64_tr_b16 v[100:101], v245 offset:4096\n"
      "v_mfma_f32_16x16x32_bf16 v[60:63], v[124:127], v[144:147], v[60:63]\n"
      "ds_read_b64_tr_b16 v[102:103], v245 offset:4608\n"
      "v_mfma_f32_16x16x32_bf16 v[64:67], v[124:127], v[148:151], v[64:67]\n"
      "ds_read_b64_tr_b16 v[104:105], v246 offset:4096\n"
      "v_mfma_f32_16x16x32_bf16 v[68:71], v[124:127], v[152:155], v[68:71]\n"
      "ds_read_b64_tr_b16 v[106:107], v246 offset:4608\n"
      "v_mfma_f32_16x16x32_bf16 v[72:75], v[124:127], v[156:159], v[72:75]\n"
      "ds_read_b64_tr_b16 v[108:109], v247 offset:4096\n"
      "v_mfma_f32_16x16x32_bf16 v[76:79], v[124:127], v[160:163], v[76:79]\n"
      "ds_read_b64_tr_b16 v[110:111], v247 offset:4608\n"
      "s_waitcnt lgkmcnt(15)\n"
      "v_mfma_f32_16x16x32_bf16 v[0:3], v[128:131], v[164:167], v[0:3]\n"
      "v_mfma_f32_16x16x32_bf16 v[4:7], v[128:131], v[168:171], v[4:7]\n"
      "v_mfma_f32_16x16x32_bf16 v[8:11], v[128:131], v[172:175], v[8:11]\n"
      "v_mfma_f32_16x16x32_bf16 v[12:15], v[128:131], v[176:179], v[12:15]\n"
      "s_waitcnt lgkmcnt(0)\n"
      "v_mfma_f32_16x16x32_bf16 v[16:19], v[128:131], v[180:183], v[16:19]\n"
      "v_mfma_f32_16x16x32_bf16 v[20:23], v[132:135], v[164:167], v[20:23]\n"
      "s_add_u32 m0, s65, 0\n"
      "s_add_u32 s57, s66, 0\n"
      "buffer_load_dwordx4 v243, s[44:47], s57 offen lds\n"
      "v_mfma_f32_16x16x32_bf16 v[24:27], v[132:135], v[168:171], v[24:27]\n"
      "v_mfma_f32_16x16x32_bf16 v[28:31], v[132:135], v[172:175], v[28:31]\n"
      "s_add_u32 m0, s65, 1024\n"
      "s_add_u32 s57, s66, 128\n"
      "buffer_load_dwordx4 v243, s[44:47], s57 offen lds\n"
      "v_mfma_f32_16x16x32_bf16 v[32:35], v[132:135], v[176:179], v[32:35]\n"
      "v_mfma_f32_16x16x32_bf16 v[36:39], v[132:135], v[180:183], v[36:39]\n"
      "s_add_u32 m0, s65, 2048\n"
      "s_add_u32 s57, s66, 256\n"
      "buffer_load_dwordx4 v243, s[44:47], s57 offen lds\n"
      "v_mfma_f32_16x16x32_bf16 v[40:43], v[136:139], v[164:167], v[40:43]\n"
      "v_mfma_f32_16x16x32_bf16 v[44:47], v[136:139], v[168:171], v[44:47]\n"
      "s_add_u32 m0, s65, 3072\n"
      "s_add_u32 s57, s66, 384\n"
      "buffer_load_dwordx4 v243, s[44:47], s57 offen lds\n"
      "v_mfma_f32_16x16x32_bf16 v[48:51], v[136:139], v[172:175], v[48:51]\n"
      "v_mfma_f32_16x16x32_bf16 v[52:55], v[136:139], v[176:179], v[52:55]\n"
      "s_add_u32 m0, s65, 4096\n"
      "s_add_u32 s57, s66, 512\n"
      "buffer_load_dwordx4 v243, s[44:47], s57 offen lds\n"
      "v_mfma_f32_16x16x32_bf16 v[56:59], v[136:139], v[180:183], v[56:59]\n"
      "v_mfma_f32_16x16x32_bf16 v[60:63], v[140:143], v[164:167], v[60:63]\n"
      "s_add_u32 m0, s65, 5120\n"
      "s_add_u32 s57, s66, 640\n"
      "buffer_load_dwordx4 v243, s[44:47], s57 offen lds\n"
      "v_mfma_f32_16x16x32_bf16 v[64:67], v[140:143], v[168:171], v[64:67]\n"
      "v_mfma_f32_16x16x32_bf16 v[68:71], v[140:143], v[172:175], v[68:71]\n"
      "s_add_u32 m0, s65, 6144\n"
      "s_add_u32 s57, s66, 768\n"
      "buffer_load_dwordx4 v243, s[44:47], s57 offen lds\n"
      "v_mfma_f32_16x16x32_bf16 v[72:75], v[140:143], v[176:179], v[72:75]\n"
      "v_mfma_f32_16x16x32_bf16 v[76:79], v[140:143], v[180:183], v[76:79]\n"
      "s_waitcnt lgkmcnt(0)\n"
      "s_barrier\n"
      "ds_read_b128 v[144:147], v248 offset:0\n"
      "ds_read_b128 v[148:151], v248 offset:2048\n"
      "ds_read_b128 v[152:155], v248 offset:4096\n"
      "ds_read_b128 v[156:159], v248 offset:6144\n"
      "ds_read_b128 v[160:163], v248 offset:8192\n"
      "ds_read_b128 v[164:167], v249 offset:0\n"
      "ds_read_b128 v[168:171], v249 offset:2048\n"
      "ds_read_b128 v[172:175], v249 offset:4096\n"
      "ds_read_b128 v[176:179], v249 offset:6144\n"
      "ds_read_b128 v[180:183], v249 offset:8192\n"
      "s_waitcnt lgkmcnt(9)\n"
      "v_mfma_f32_16x16x32_bf16 v[0:3], v[80:83], v[144:147], v[0:3]\n"
      "s_waitcnt lgkmcnt(8)\n"
      "v_mfma_f32_16x16x32_bf16 v[4:7], v[80:83], v[148:151], v[4:7]\n"
      "s_waitcnt lgkmcnt(7)\n"
      "v_mfma_f32_16x16x32_bf16 v[8:11], v[80:83], v[152:155], v[8:11]\n"
      "s_waitcnt lgkmcnt(6)\n"
      "v_mfma_f32_16x16x32_bf16 v[12:15], v[80:83], v[156:159], v[12:15]\n"
      "s_waitcnt lgkmcnt(5)\n"
      "v_mfma_f32_16x16x32_bf16 v[16:19], v[80:83], v[160:163], v[16:19]\n"
      "v_mfma_f32_16x16x32_bf16 v[20:23], v[84:87], v[144:147], v[20:23]\n"
      "s_waitcnt vmcnt(7)\n"
      "ds_read_b64_tr_b16 v[112:113], v244 offset:32768\n"
      "v_mfma_f32_16x16x32_bf16 v[24:27], v[84:87], v[148:151], v[24:27]\n"
      "ds_read_b64_tr_b16 v[114:115], v244 offset:33280\n"
      "v_mfma_f32_16x16x32_bf16 v[28:31], v[84:87], v[152:155], v[28:31]\n"
      "ds_read_b64_tr_b16 v[116:117], v245 offset:32768\n"
      "v_mfma_f32_16x16x32_bf16 v[32:35], v[84:87], v[156:159], v[32:35]\n"
      "ds_read_b64_tr_b16 v[118:119], v245 offset:33280\n"
      "v_mfma_f32_16x16x32_bf16 v[36:39], v[84:87], v[160:163], v[36:39]\n"
      "ds_read_b64_tr_b16 v[120:121], v246 offset:32768\n"
      "v_mfma_f32_16x16x32_bf16 v[40:43], v[88:91], v[144:147], v[40:43]\n"
      "ds_read_b64_tr_b16 v[122:123], v246 offset:33280\n"
      "v_mfma_f32_16x16x32_bf16 v[44:47], v[88:91], v[148:151], v[44:47]\n"
      "ds_read_b64_tr_b16 v[124:125], v247 offset:32768\n"
      "v_mfma_f32_16x16x32_bf16 v[48:51], v[88:91], v[152:155], v[48:51]\n"
      "ds_read_b64_tr_b16 v[126:127], v247 offset:33280\n"
      "ds_read_b64_tr_b16 v[128:129], v244 offset:36864\n"
      "v_mfma_f32_16x16x32_bf16 v[52:55], v[88:91], v[156:159], v[52:55]\n"
      "ds_read_b64_tr_b16 v[130:131], v244 offset:37376\n"
      "v_mfma_f32_16x16x32_bf16 v[56:59], v[88:91], v[160:163], v[56:59]\n"
      "ds_read_b64_tr_b16 v[132:133], v245 offset:36864\n"
      "v_mfma_f32_16x16x32_bf16 v[60:63], v[92:95], v[144:147], v[60:63]\n"
      "ds_read_b64_tr_b16 v[134:135], v245 offset:37376\n"
      "v_mfma_f32_16x16x32_bf16 v[64:67], v[92:95], v[148:151], v[64:67]\n"
      "ds_read_b64_tr_b16 v[136:137], v246 offset:36864\n"
      "v_mfma_f32_16x16x32_bf16 v[68:71], v[92:95], v[152:155], v[68:71]\n"
      "ds_read_b64_tr_b16 v[138:139], v246 offset:37376\n"
      "v_mfma_f32_16x16x32_bf16 v[72:75], v[92:95], v[156:159], v[72:75]\n"
      "ds_read_b64_tr_b16 v[140:141], v247 offset:36864\n"
      "v_mfma_f32_16x16x32_bf16 v[76:79], v[92:95], v[160:163], v[76:79]\n"
      "ds_read_b64_tr_b16 v[142:143], v247 offset:37376\n"
      "s_waitcnt lgkmcnt(15)\n"
      "v_mfma_f32_16x16x32_bf16 v[0:3], v[96:99], v[164:167], v[0:3]\n"
      "v_mfma_f32_16x16x32_bf16 v[4:7], v[96:99], v[168:171], v[4:7]\n"
      "v_mfma_f32_16x16x32_bf16 v[8:11], v[96:99], v[172:175], v[8:11]\n"
      "v_mfma_f32_16x16x32_bf16 v[12:15], v[96:99], v[176:179], v[12:15]\n"
      "s_waitcnt lgkmcnt(0)\n"
      "v_mfma_f32_16x16x32_bf16 v[16:19], v[96:99], v[180:183], v[16:19]\n"
      "v_mfma_f32_16x16x32_bf16 v[20:23], v[100:103], v[164:167], v[20:23]\n"
      "s_add_u32 m0, s65, 28672\n"
      "s_add_u32 s57, s66, 28672\n"
      "buffer_load_dwordx4 v243, s[44:47], s57 offen lds\n"
      "v_mfma_f32_16x16x32_bf16 v[24:27], v[100:103], v[168:171], v[24:27]\n"
      "v_mfma_f32_16x16x32_bf16 v[28:31], v[100:103], v[172:175], v[28:31]\n"
      "s_add_u32 m0, s65, 29696\n"
      "s_add_u32 s57, s66, 28800\n"
      "buffer_load_dwordx4 v243, s[44:47], s57 offen lds\n"
      "v_mfma_f32_16x16x32_bf16 v[32:35], v[100:103], v[176:179], v[32:35]\n"
      "v_mfma_f32_16x16x32_bf16 v[36:39], v[100:103], v[180:183], v[36:39]\n"
      "s_add_u32 m0, s65, 30720\n"
      "s_add_u32 s57, s66, 28928\n"
      "buffer_load_dwordx4 v243, s[44:47], s57 offen lds\n"
      "v_mfma_f32_16x16x32_bf16 v[40:43], v[104:107], v[164:167], v[40:43]\n"
      "v_mfma_f32_16x16x32_bf16 v[44:47], v[104:107], v[168:171], v[44:47]\n"
      "s_add_u32 m0, s65, 31744\n"
      "s_add_u32 s57, s66, 29056\n"
      "buffer_load_dwordx4 v243, s[44:47], s57 offen lds\n"
      "v_mfma_f32_16x16x32_bf16 v[48:51], v[104:107], v[172:175], v[48:51]\n"
      "v_mfma_f32_16x16x32_bf16 v[52:55], v[104:107], v[176:179], v[52:55]\n"
      "s_add_u32 m0, s65, 32768\n"
      "s_add_u32 s57, s66, 29184\n"
      "buffer_load_dwordx4 v243, s[44:47], s57 offen lds\n"
      "v_mfma_f32_16x16x32_bf16 v[56:59], v[104:107], v[180:183], v[56:59]\n"
      "v_mfma_f32_16x16x32_bf16 v[60:63], v[108:111], v[164:167], v[60:63]\n"
      "s_add_u32 m0, s65, 33792\n"
      "s_add_u32 s57, s66, 29312\n"
      "buffer_load_dwordx4 v243, s[44:47], s57 offen lds\n"
      "v_mfma_f32_16x16x32_bf16 v[64:67], v[108:111], v[168:171], v[64:67]\n"
      "v_mfma_f32_16x16x32_bf16 v[68:71], v[108:111], v[172:175], v[68:71]\n"
      "s_add_u32 m0, s65, 34816\n"
      "s_add_u32 s57, s66, 29440\n"
      "buffer_load_dwordx4 v243, s[44:47], s57 offen lds\n"
      "v_mfma_f32_16x16x32_bf16 v[72:75], v[108:111], v[176:179], v[72:75]\n"
      "v_mfma_f32_16x16x32_bf16 v[76:79], v[108:111], v[180:183], v[76:79]\n"
      "s_waitcnt lgkmcnt(0)\n"
      "s_barrier\n"
      "s_mul_i32 s58, s52, 10240\n"
      "s_add_u32 s58, s58, 120320\n"
      "v_lshrrev_b32 v244, 1, v252\n"
      "v_xor_b32 v244, v244, v254\n"
      "v_lshlrev_b32 v244, 4, v244\n"
      "v_and_b32 v255, 1, v252\n"
      "v_lshl_or_b32 v244, v255, 3, v244\n"
      "v_lshl_add_u32 v244, v253, 7, v244\n"
      "v_add_u32 v244, s58, v244\n"
      "v_xor_b32 v245, 32, v244\n"
      "v_xor_b32 v246, 64, v244\n"
      "v_xor_b32 v247, 96, v244\n"
      "ds_read_b128 v[144:147], v248 offset:10240\n"
      "ds_read_b128 v[148:151], v248 offset:12288\n"
      "ds_read_b128 v[152:155], v248 offset:14336\n"
      "ds_read_b128 v[156:159], v248 offset:16384\n"
      "ds_read_b128 v[160:163], v248 offset:18432\n"
      "ds_read_b128 v[164:167], v249 offset:10240\n"
      "ds_read_b128 v[168:171], v249 offset:12288\n"
      "ds_read_b128 v[172:175], v249 offset:14336\n"
      "ds_read_b128 v[176:179], v249 offset:16384\n"
      "ds_read_b128 v[180:183], v249 offset:18432\n"
      "s_waitcnt lgkmcnt(9)\n"
      "v_mfma_f32_16x16x32_bf16 v[0:3], v[112:115], v[144:147], v[0:3]\n"
      "s_waitcnt lgkmcnt(8)\n"
      "v_mfma_f32_16x16x32_bf16 v[4:7], v[112:115], v[148:151], v[4:7]\n"
      "s_waitcnt lgkmcnt(7)\n"
      "v_mfma_f32_16x16x32_bf16 v[8:11], v[112:115], v[152:155], v[8:11]\n"
      "s_waitcnt lgkmcnt(6)\n"
      "v_mfma_f32_16x16x32_bf16 v[12:15], v[112:115], v[156:159], v[12:15]\n"
      "s_waitcnt lgkmcnt(5)\n"
      "v_mfma_f32_16x16x32_bf16 v[16:19], v[112:115], v[160:163], v[16:19]\n"
      "v_mfma_f32_16x16x32_bf16 v[20:23], v[116:119], v[144:147], v[20:23]\n"
      "v_mfma_f32_16x16x32_bf16 v[24:27], v[116:119], v[148:151], v[24:27]\n"
      "v_mfma_f32_16x16x32_bf16 v[28:31], v[116:119], v[152:155], v[28:31]\n"
      "v_mfma_f32_16x16x32_bf16 v[32:35], v[116:119], v[156:159], v[32:35]\n"
      "v_mfma_f32_16x16x32_bf16 v[36:39], v[116:119], v[160:163], v[36:39]\n"
      "v_mfma_f32_16x16x32_bf16 v[40:43], v[120:123], v[144:147], v[40:43]\n"
      "v_mfma_f32_16x16x32_bf16 v[44:47], v[120:123], v[148:151], v[44:47]\n"
      "v_mfma_f32_16x16x32_bf16 v[48:51], v[120:123], v[152:155], v[48:51]\n"
      "v_mfma_f32_16x16x32_bf16 v[52:55], v[120:123], v[156:159], v[52:55]\n"
      "v_mfma_f32_16x16x32_bf16 v[56:59], v[120:123], v[160:163], v[56:59]\n"
      "v_mfma_f32_16x16x32_bf16 v[60:63], v[124:127], v[144:147], v[60:63]\n"
      "v_mfma_f32_16x16x32_bf16 v[64:67], v[124:127], v[148:151], v[64:67]\n"
      "v_mfma_f32_16x16x32_bf16 v[68:71], v[124:127], v[152:155], v[68:71]\n"
      "v_mfma_f32_16x16x32_bf16 v[72:75], v[124:127], v[156:159], v[72:75]\n"
      "v_mfma_f32_16x16x32_bf16 v[76:79], v[124:127], v[160:163], v[76:79]\n"
      "s_waitcnt lgkmcnt(4)\n"
      "v_mfma_f32_16x16x32_bf16 v[0:3], v[128:131], v[164:167], v[0:3]\n"
      "s_waitcnt lgkmcnt(3)\n"
      "v_mfma_f32_16x16x32_bf16 v[4:7], v[128:131], v[168:171], v[4:7]\n"
      "s_waitcnt lgkmcnt(2)\n"
      "v_mfma_f32_16x16x32_bf16 v[8:11], v[128:131], v[172:175], v[8:11]\n"
      "s_waitcnt lgkmcnt(1)\n"
      "v_mfma_f32_16x16x32_bf16 v[12:15], v[128:131], v[176:179], v[12:15]\n"
      "s_add_u32 m0, s65, 57344\n"
      "s_add_u32 s57, s66, 57344\n"
      "buffer_load_dwordx4 v243, s[44:47], s57 offen lds\n"
      "s_waitcnt lgkmcnt(0)\n"
      "v_mfma_f32_16x16x32_bf16 v[16:19], v[128:131], v[180:183], v[16:19]\n"
      "s_add_u32 m0, s65, 58368\n"
      "s_add_u32 s57, s66, 57472\n"
      "buffer_load_dwordx4 v243, s[44:47], s57 offen lds\n"
      "v_mfma_f32_16x16x32_bf16 v[20:23], v[132:135], v[164:167], v[20:23]\n"
      "s_add_u32 m0, s65, 59392\n"
      "s_add_u32 s57, s66, 57600\n"
      "buffer_load_dwordx4 v243, s[44:47], s57 offen lds\n"
      "v_mfma_f32_16x16x32_bf16 v[24:27], v[132:135], v[168:171], v[24:27]\n"
      "s_add_u32 m0, s65, 60416\n"
      "s_add_u32 s57, s66, 57728\n"
      "buffer_load_dwordx4 v243, s[44:47], s57 offen lds\n"
      "v_mfma_f32_16x16x32_bf16 v[28:31], v[132:135], v[172:175], v[28:31]\n"
      "s_add_u32 m0, s65, 61440\n"
      "s_add_u32 s57, s66, 57856\n"
      "buffer_load_dwordx4 v243, s[44:47], s57 offen lds\n"
      "v_mfma_f32_16x16x32_bf16 v[32:35], v[132:135], v[176:179], v[32:35]\n"
      "v_mfma_f32_16x16x32_bf16 v[36:39], v[132:135], v[180:183], v[36:39]\n"
      "s_add_u32 m0, s65, 62464\n"
      "s_add_u32 s57, s66, 57984\n"
      "buffer_load_dwordx4 v243, s[44:47], s57 offen lds\n"
      "v_mfma_f32_16x16x32_bf16 v[40:43], v[136:139], v[164:167], v[40:43]\n"
      "s_add_u32 m0, s65, 63488\n"
      "s_add_u32 s57, s66, 58112\n"
      "buffer_load_dwordx4 v243, s[44:47], s57 offen lds\n"
      "v_mfma_f32_16x16x32_bf16 v[44:47], v[136:139], v[168:171], v[44:47]\n"
      "s_add_u32 m0, s65, 86016\n"
      "s_add_u32 s57, s66, 86016\n"
      "buffer_load_dwordx4 v243, s[44:47], s57 offen lds\n"
      "v_mfma_f32_16x16x32_bf16 v[48:51], v[136:139], v[172:175], v[48:51]\n"
      "s_add_u32 m0, s65, 87040\n"
      "s_add_u32 s57, s66, 86144\n"
      "buffer_load_dwordx4 v243, s[44:47], s57 offen lds\n"
      "v_mfma_f32_16x16x32_bf16 v[52:55], v[136:139], v[176:179], v[52:55]\n"
      "s_add_u32 m0, s65, 88064\n"
      "s_add_u32 s57, s66, 86272\n"
      "buffer_load_dwordx4 v243, s[44:47], s57 offen lds\n"
      "v_mfma_f32_16x16x32_bf16 v[56:59], v[136:139], v[180:183], v[56:59]\n"
      "v_mfma_f32_16x16x32_bf16 v[60:63], v[140:143], v[164:167], v[60:63]\n"
      "s_add_u32 m0, s65, 89088\n"
      "s_add_u32 s57, s66, 86400\n"
      "buffer_load_dwordx4 v243, s[44:47], s57 offen lds\n"
      "v_mfma_f32_16x16x32_bf16 v[64:67], v[140:143], v[168:171], v[64:67]\n"
      "s_add_u32 m0, s65, 90112\n"
      "s_add_u32 s57, s66, 86528\n"
      "buffer_load_dwordx4 v243, s[44:47], s57 offen lds\n"
      "v_mfma_f32_16x16x32_bf16 v[68:71], v[140:143], v[172:175], v[68:71]\n"
      "s_add_u32 m0, s65, 91136\n"
      "s_add_u32 s57, s66, 86656\n"
      "buffer_load_dwordx4 v243, s[44:47], s57 offen lds\n"
      "v_mfma_f32_16x16x32_bf16 v[72:75], v[140:143], v[176:179], v[72:75]\n"
      "s_add_u32 m0, s65, 92160\n"
      "s_add_u32 s57, s66, 86784\n"
      "buffer_load_dwordx4 v243, s[44:47], s57 offen lds\n"
      "v_mfma_f32_16x16x32_bf16 v[76:79], v[140:143], v[180:183], v[76:79]\n"
      "s_waitcnt vmcnt(21)\n"
      "s_waitcnt lgkmcnt(0)\n"
      "s_barrier\n"
      "v_lshlrev_b32 v255, 4, v253\n"
      "v_add_u32 v255, 162560, v255\n"
      "ds_read_b128 v[80:83], v255 offset:0\n"
      "ds_read_b128 v[84:87], v255 offset:256\n"
      "ds_read_b128 v[88:91], v255 offset:512\n"
      "ds_read_b128 v[92:95], v255 offset:768\n"
      "ds_read_b128 v[96:99], v255 offset:1024\n"
      "s_mov_b32 s59, 0x76800000\n"
      "v_mov_b32 v100, 0x7fc00000\n"
      "s_waitcnt lgkmcnt(4)\n"
      "v_add_f32 v80, v80, v81\n"
      "v_add_f32 v82, v82, v83\n"
      "v_add_f32 v80, v80, v82\n"
      "v_rcp_f32 v81, v80\n"
      "v_cmp_ge_f32 vcc, s59, v80\n"
      "s_nop 1\n"
      "v_cndmask_b32 v80, v100, v81, vcc\n"
      "v_pk_mul_f32 v[0:1], v[0:1], v[80:81] op_sel_hi:[1,0]\n"
      "v_pk_mul_f32 v[2:3], v[2:3], v[80:81] op_sel_hi:[1,0]\n"
      "v_cvt_pk_bf16_f32 v104, v0, v1\n"
      "v_cvt_pk_bf16_f32 v105, v2, v3\n"
      "ds_write_b64 v244, v[104:105] offset:0\n"
      "v_pk_mul_f32 v[20:21], v[20:21], v[80:81] op_sel_hi:[1,0]\n"
      "v_pk_mul_f32 v[22:23], v[22:23], v[80:81] op_sel_hi:[1,0]\n"
      "v_cvt_pk_bf16_f32 v106, v20, v21\n"
      "v_cvt_pk_bf16_f32 v107, v22, v23\n"
      "ds_write_b64 v245, v[106:107] offset:0\n"
      "v_pk_mul_f32 v[40:41], v[40:41], v[80:81] op_sel_hi:[1,0]\n"
      "v_pk_mul_f32 v[42:43], v[42:43], v[80:81] op_sel_hi:[1,0]\n"
      "v_cvt_pk_bf16_f32 v108, v40, v41\n"
      "v_cvt_pk_bf16_f32 v109, v42, v43\n"
      "ds_write_b64 v246, v[108:109] offset:0\n"
      "v_pk_mul_f32 v[60:61], v[60:61], v[80:81] op_sel_hi:[1,0]\n"
      "v_pk_mul_f32 v[62:63], v[62:63], v[80:81] op_sel_hi:[1,0]\n"
      "v_cvt_pk_bf16_f32 v110, v60, v61\n"
      "v_cvt_pk_bf16_f32 v111, v62, v63\n"
      "ds_write_b64 v247, v[110:111] offset:0\n"
      "s_waitcnt lgkmcnt(7)\n"
      "v_add_f32 v84, v84, v85\n"
      "v_add_f32 v86, v86, v87\n"
      "v_add_f32 v84, v84, v86\n"
      "v_rcp_f32 v85, v84\n"
      "v_cmp_ge_f32 vcc, s59, v84\n"
      "s_nop 1\n"
      "v_cndmask_b32 v84, v100, v85, vcc\n"
      "v_pk_mul_f32 v[4:5], v[4:5], v[84:85] op_sel_hi:[1,0]\n"
      "v_pk_mul_f32 v[6:7], v[6:7], v[84:85] op_sel_hi:[1,0]\n"
      "v_cvt_pk_bf16_f32 v112, v4, v5\n"
      "v_cvt_pk_bf16_f32 v113, v6, v7\n"
      "ds_write_b64 v244, v[112:113] offset:2048\n"
      "v_pk_mul_f32 v[24:25], v[24:25], v[84:85] op_sel_hi:[1,0]\n"
      "v_pk_mul_f32 v[26:27], v[26:27], v[84:85] op_sel_hi:[1,0]\n"
      "v_cvt_pk_bf16_f32 v114, v24, v25\n"
      "v_cvt_pk_bf16_f32 v115, v26, v27\n"
      "ds_write_b64 v245, v[114:115] offset:2048\n"
      "v_pk_mul_f32 v[44:45], v[44:45], v[84:85] op_sel_hi:[1,0]\n"
      "v_pk_mul_f32 v[46:47], v[46:47], v[84:85] op_sel_hi:[1,0]\n"
      "v_cvt_pk_bf16_f32 v116, v44, v45\n"
      "v_cvt_pk_bf16_f32 v117, v46, v47\n"
      "ds_write_b64 v246, v[116:117] offset:2048\n"
      "v_pk_mul_f32 v[64:65], v[64:65], v[84:85] op_sel_hi:[1,0]\n"
      "v_pk_mul_f32 v[66:67], v[66:67], v[84:85] op_sel_hi:[1,0]\n"
      "v_cvt_pk_bf16_f32 v118, v64, v65\n"
      "v_cvt_pk_bf16_f32 v119, v66, v67\n"
      "ds_write_b64 v247, v[118:119] offset:2048\n"
      "s_waitcnt lgkmcnt(10)\n"
      "v_add_f32 v88, v88, v89\n"
      "v_add_f32 v90, v90, v91\n"
      "v_add_f32 v88, v88, v90\n"
      "v_rcp_f32 v89, v88\n"
      "v_cmp_ge_f32 vcc, s59, v88\n"
      "s_nop 1\n"
      "v_cndmask_b32 v88, v100, v89, vcc\n"
      "v_pk_mul_f32 v[8:9], v[8:9], v[88:89] op_sel_hi:[1,0]\n"
      "v_pk_mul_f32 v[10:11], v[10:11], v[88:89] op_sel_hi:[1,0]\n"
      "v_cvt_pk_bf16_f32 v104, v8, v9\n"
      "v_cvt_pk_bf16_f32 v105, v10, v11\n"
      "ds_write_b64 v244, v[104:105] offset:4096\n"
      "v_pk_mul_f32 v[28:29], v[28:29], v[88:89] op_sel_hi:[1,0]\n"
      "v_pk_mul_f32 v[30:31], v[30:31], v[88:89] op_sel_hi:[1,0]\n"
      "v_cvt_pk_bf16_f32 v106, v28, v29\n"
      "v_cvt_pk_bf16_f32 v107, v30, v31\n"
      "ds_write_b64 v245, v[106:107] offset:4096\n"
      "v_pk_mul_f32 v[48:49], v[48:49], v[88:89] op_sel_hi:[1,0]\n"
      "v_pk_mul_f32 v[50:51], v[50:51], v[88:89] op_sel_hi:[1,0]\n"
      "v_cvt_pk_bf16_f32 v108, v48, v49\n"
      "v_cvt_pk_bf16_f32 v109, v50, v51\n"
      "ds_write_b64 v246, v[108:109] offset:4096\n"
      "v_pk_mul_f32 v[68:69], v[68:69], v[88:89] op_sel_hi:[1,0]\n"
      "v_pk_mul_f32 v[70:71], v[70:71], v[88:89] op_sel_hi:[1,0]\n"
      "v_cvt_pk_bf16_f32 v110, v68, v69\n"
      "v_cvt_pk_bf16_f32 v111, v70, v71\n"
      "ds_write_b64 v247, v[110:111] offset:4096\n"
      "s_waitcnt lgkmcnt(13)\n"
      "v_add_f32 v92, v92, v93\n"
      "v_add_f32 v94, v94, v95\n"
      "v_add_f32 v92, v92, v94\n"
      "v_rcp_f32 v93, v92\n"
      "v_cmp_ge_f32 vcc, s59, v92\n"
      "s_nop 1\n"
      "v_cndmask_b32 v92, v100, v93, vcc\n"
      "v_pk_mul_f32 v[12:13], v[12:13], v[92:93] op_sel_hi:[1,0]\n"
      "v_pk_mul_f32 v[14:15], v[14:15], v[92:93] op_sel_hi:[1,0]\n"
      "v_cvt_pk_bf16_f32 v112, v12, v13\n"
      "v_cvt_pk_bf16_f32 v113, v14, v15\n"
      "ds_write_b64 v244, v[112:113] offset:6144\n"
      "v_pk_mul_f32 v[32:33], v[32:33], v[92:93] op_sel_hi:[1,0]\n"
      "v_pk_mul_f32 v[34:35], v[34:35], v[92:93] op_sel_hi:[1,0]\n"
      "v_cvt_pk_bf16_f32 v114, v32, v33\n"
      "v_cvt_pk_bf16_f32 v115, v34, v35\n"
      "ds_write_b64 v245, v[114:115] offset:6144\n"
      "v_pk_mul_f32 v[52:53], v[52:53], v[92:93] op_sel_hi:[1,0]\n"
      "v_pk_mul_f32 v[54:55], v[54:55], v[92:93] op_sel_hi:[1,0]\n"
      "v_cvt_pk_bf16_f32 v116, v52, v53\n"
      "v_cvt_pk_bf16_f32 v117, v54, v55\n"
      "ds_write_b64 v246, v[116:117] offset:6144\n"
      "v_pk_mul_f32 v[72:73], v[72:73], v[92:93] op_sel_hi:[1,0]\n"
      "v_pk_mul_f32 v[74:75], v[74:75], v[92:93] op_sel_hi:[1,0]\n"
      "v_cvt_pk_bf16_f32 v118, v72, v73\n"
      "v_cvt_pk_bf16_f32 v119, v74, v75\n"
      "ds_write_b64 v247, v[118:119] offset:6144\n"
      "s_waitcnt lgkmcnt(15)\n"
      "v_add_f32 v96, v96, v97\n"
      "v_add_f32 v98, v98, v99\n"
      "v_add_f32 v96, v96, v98\n"
      "v_rcp_f32 v97, v96\n"
      "v_cmp_ge_f32 vcc, s59, v96\n"
      "s_nop 1\n"
      "v_cndmask_b32 v96, v100, v97, vcc\n"
      "v_pk_mul_f32 v[16:17], v[16:17], v[96:97] op_sel_hi:[1,0]\n"
      "v_pk_mul_f32 v[18:19], v[18:19], v[96:97] op_sel_hi:[1,0]\n"
      "v_cvt_pk_bf16_f32 v104, v16, v17\n"
      "v_cvt_pk_bf16_f32 v105, v18, v19\n"
      "ds_write_b64 v244, v[104:105] offset:8192\n"
      "v_pk_mul_f32 v[36:37], v[36:37], v[96:97] op_sel_hi:[1,0]\n"
      "v_pk_mul_f32 v[38:39], v[38:39], v[96:97] op_sel_hi:[1,0]\n"
      "v_cvt_pk_bf16_f32 v106, v36, v37\n"
      "v_cvt_pk_bf16_f32 v107, v38, v39\n"
      "ds_write_b64 v245, v[106:107] offset:8192\n"
      "v_pk_mul_f32 v[56:57], v[56:57], v[96:97] op_sel_hi:[1,0]\n"
      "v_pk_mul_f32 v[58:59], v[58:59], v[96:97] op_sel_hi:[1,0]\n"
      "v_cvt_pk_bf16_f32 v108, v56, v57\n"
      "v_cvt_pk_bf16_f32 v109, v58, v59\n"
      "ds_write_b64 v246, v[108:109] offset:8192\n"
      "v_pk_mul_f32 v[76:77], v[76:77], v[96:97] op_sel_hi:[1,0]\n"
      "v_pk_mul_f32 v[78:79], v[78:79], v[96:97] op_sel_hi:[1,0]\n"
      "v_cvt_pk_bf16_f32 v110, v76, v77\n"
      "v_cvt_pk_bf16_f32 v111, v78, v79\n"
      "ds_write_b64 v247, v[110:111] offset:8192\n"
      "v_bfe_u32 v236, v251, 3, 1\n"
      "v_and_b32 v255, 1, v252\n"
      "v_lshl_or_b32 v236, v255, 1, v236\n"
      "v_bfe_u32 v255, v251, 1, 1\n"
      "v_lshl_or_b32 v236, v236, 1, v255\n"
      "v_lshlrev_b32 v236, 4, v236\n"
      "v_and_b32 v255, 1, v251\n"
      "v_lshl_or_b32 v236, v255, 3, v236\n"
      "v_bfe_u32 v255, v251, 2, 2\n"
      "v_lshl_or_b32 v236, v255, 7, v236\n"
      "v_mul_u32_u24 v255, 7168, v252\n"
      "v_add_u32 v236, v255, v236\n"
      "s_mul_i32 s57, s52, 7\n"
      "s_add_u32 s57, s57, 4\n"
      "s_and_b32 s58, s57, 3\n"
      "s_lshl_b32 s58, s58, 5\n"
      "v_xor_b32 v232, s58, v236\n"
      "s_lshr_b32 s57, s57, 2\n"
      "s_lshl_b32 s57, s57, 10\n"
      "v_add_u32 v232, s57, v232\n"
      "s_mul_i32 s57, s52, 7\n"
      "s_add_u32 s57, s57, 5\n"
      "s_and_b32 s58, s57, 3\n"
      "s_lshl_b32 s58, s58, 5\n"
      "v_xor_b32 v233, s58, v236\n"
      "s_lshr_b32 s57, s57, 2\n"
      "s_lshl_b32 s57, s57, 10\n"
      "v_add_u32 v233, s57, v233\n"
      "s_mul_i32 s57, s52, 7\n"
      "s_add_u32 s57, s57, 6\n"
      "s_and_b32 s58, s57, 3\n"
      "s_lshl_b32 s58, s58, 5\n"
      "v_xor_b32 v234, s58, v236\n"
      "s_lshr_b32 s57, s57, 2\n"
      "s_lshl_b32 s57, s57, 10\n"
      "v_add_u32 v234, s57, v234\n"
      "v_add_u32 v236, 57344, v232\n"
      "v_add_u32 v237, 57344, v233\n"
      "v_add_u32 v238, 57344, v234\n"
      "v_xor_b32 v248, v252, v254\n"
      "v_lshlrev_b32 v248, 4, v248\n"
      "v_lshl_add_u32 v248, v253, 7, v248\n"
      "v_add_u32 v248, 120320, v248\n"
      "v_xor_b32 v249, 64, v248\n"
      "ds_read_b64_tr_b16 v[160:161], v232 offset:0\n"
      "ds_read_b64_tr_b16 v[162:163], v232 offset:512\n"
      "ds_read_b64_tr_b16 v[164:165], v233 offset:0\n"
      "ds_read_b64_tr_b16 v[166:167], v233 offset:512\n"
      "ds_read_b64_tr_b16 v[168:169], v234 offset:0\n"
      "ds_read_b64_tr_b16 v[170:171], v234 offset:512\n"
      "s_waitcnt vmcnt(14)\n"
      "s_waitcnt lgkmcnt(0)\n"
      "s_barrier\n"
      "ds_read_b128 v[192:195], v248 offset:0\n"
      "ds_read_b128 v[196:199], v248 offset:2048\n"
      "ds_read_b128 v[200:203], v248 offset:4096\n"
      "ds_read_b128 v[204:207], v248 offset:6144\n"
      "ds_read_b128 v[208:211], v248 offset:8192\n"
      "s_waitcnt lgkmcnt(0)\n"
      "v_mfma_f32_16x16x32_bf16 v[0:3], v[160:163], v[192:195], 0\n"
      "ds_read_b64_tr_b16 v[176:177], v232 offset:28672\n"
      "s_add_u32 m0, s65, 0\n"
      "s_add_u32 s57, s66, 114688\n"
      "buffer_load_dwordx4 v243, s[44:47], s57 offen lds\n"
      "v_mfma_f32_16x16x32_bf16 v[4:7], v[160:163], v[196:199], 0\n"
      "ds_read_b64_tr_b16 v[178:179], v232 offset:29184\n"
      "v_mfma_f32_16x16x32_bf16 v[8:11], v[160:163], v[200:203], 0\n"
      "ds_read_b64_tr_b16 v[180:181], v233 offset:28672\n"
      "s_add_u32 m0, s65, 1024\n"
      "s_add_u32 s57, s66, 114816\n"
      "buffer_load_dwordx4 v243, s[44:47], s57 offen lds\n"
      "v_mfma_f32_16x16x32_bf16 v[12:15], v[160:163], v[204:207], 0\n"
      "ds_read_b64_tr_b16 v[182:183], v233 offset:29184\n"
      "v_mfma_f32_16x16x32_bf16 v[16:19], v[160:163], v[208:211], 0\n"
      "ds_read_b64_tr_b16 v[184:185], v234 offset:28672\n"
      "s_add_u32 m0, s65, 2048\n"
      "s_add_u32 s57, s66, 114944\n"
      "buffer_load_dwordx4 v243, s[44:47], s57 offen lds\n"
      "v_mfma_f32_16x16x32_bf16 v[20:23], v[164:167], v[192:195], 0\n"
      "ds_read_b64_tr_b16 v[186:187], v234 offset:29184\n"
      "v_mfma_f32_16x16x32_bf16 v[24:27], v[164:167], v[196:199], 0\n"
      "ds_read_b128 v[212:215], v249 offset:0\n"
      "s_add_u32 m0, s65, 3072\n"
      "s_add_u32 s57, s66, 115072\n"
      "buffer_load_dwordx4 v243, s[44:47], s57 offen lds\n"
      "v_mfma_f32_16x16x32_bf16 v[28:31], v[164:167], v[200:203], 0\n"
      "ds_read_b128 v[216:219], v249 offset:2048\n"
      "v_mfma_f32_16x16x32_bf16 v[32:35], v[164:167], v[204:207], 0\n"
      "ds_read_b128 v[220:223], v249 offset:4096\n"
      "s_add_u32 m0, s65, 4096\n"
      "s_add_u32 s57, s66, 115200\n"
      "buffer_load_dwordx4 v243, s[44:47], s57 offen lds\n"
      "v_mfma_f32_16x16x32_bf16 v[36:39], v[164:167], v[208:211], 0\n"
      "ds_read_b128 v[224:227], v249 offset:6144\n"
      "v_mfma_f32_16x16x32_bf16 v[40:43], v[168:171], v[192:195], 0\n"
      "ds_read_b128 v[228:231], v249 offset:8192\n"
      "s_add_u32 m0, s65, 5120\n"
      "s_add_u32 s57, s66, 115328\n"
      "buffer_load_dwordx4 v243, s[44:47], s57 offen lds\n"
      "v_mfma_f32_16x16x32_bf16 v[44:47], v[168:171], v[196:199], 0\n"
      "v_mfma_f32_16x16x32_bf16 v[48:51], v[168:171], v[200:203], 0\n"
      "s_add_u32 m0, s65, 6144\n"
      "s_add_u32 s57, s66, 115456\n"
      "buffer_load_dwordx4 v243, s[44:47], s57 offen lds\n"
      "v_mfma_f32_16x16x32_bf16 v[52:55], v[168:171], v[204:207], 0\n"
      "v_mfma_f32_16x16x32_bf16 v[56:59], v[168:171], v[208:211], 0\n"
      "s_waitcnt vmcnt(14)\n"
      "s_waitcnt lgkmcnt(0)\n"
      "s_barrier\n"
      "v_mfma_f32_16x16x32_bf16 v[0:3], v[176:179], v[212:215], v[0:3]\n"
      "ds_read_b64_tr_b16 v[160:161], v236 offset:0\n"
      "s_add_u32 m0, s65, 28672\n"
      "s_add_u32 s57, s66, 143360\n"
      "buffer_load_dwordx4 v243, s[44:47], s57 offen lds\n"
      "v_mfma_f32_16x16x32_bf16 v[4:7], v[176:179], v[216:219], v[4:7]\n"
      "ds_read_b64_tr_b16 v[162:163], v236 offset:512\n"
      "v_mfma_f32_16x16x32_bf16 v[8:11], v[176:179], v[220:223], v[8:11]\n"
      "ds_read_b64_tr_b16 v[164:165], v237 offset:0\n"
      "s_add_u32 m0, s65, 29696\n"
      "s_add_u32 s57, s66, 143488\n"
      "buffer_load_dwordx4 v243, s[44:47], s57 offen lds\n"
      "v_mfma_f32_16x16x32_bf16 v[12:15], v[176:179], v[224:227], v[12:15]\n"
      "ds_read_b64_tr_b16 v[166:167], v237 offset:512\n"
      "v_mfma_f32_16x16x32_bf16 v[16:19], v[176:179], v[228:231], v[16:19]\n"
      "ds_read_b64_tr_b16 v[168:169], v238 offset:0\n"
      "s_add_u32 m0, s65, 30720\n"
      "s_add_u32 s57, s66, 143616\n"
      "buffer_load_dwordx4 v243, s[44:47], s57 offen lds\n"
      "v_mfma_f32_16x16x32_bf16 v[20:23], v[180:183], v[212:215], v[20:23]\n"
      "ds_read_b64_tr_b16 v[170:171], v238 offset:512\n"
      "v_mfma_f32_16x16x32_bf16 v[24:27], v[180:183], v[216:219], v[24:27]\n"
      "ds_read_b128 v[192:195], v248 offset:10240\n"
      "s_add_u32 m0, s65, 31744\n"
      "s_add_u32 s57, s66, 143744\n"
      "buffer_load_dwordx4 v243, s[44:47], s57 offen lds\n"
      "v_mfma_f32_16x16x32_bf16 v[28:31], v[180:183], v[220:223], v[28:31]\n"
      "ds_read_b128 v[196:199], v248 offset:12288\n"
      "v_mfma_f32_16x16x32_bf16 v[32:35], v[180:183], v[224:227], v[32:35]\n"
      "ds_read_b128 v[200:203], v248 offset:14336\n"
      "s_add_u32 m0, s65, 32768\n"
      "s_add_u32 s57, s66, 143872\n"
      "buffer_load_dwordx4 v243, s[44:47], s57 offen lds\n"
      "v_mfma_f32_16x16x32_bf16 v[36:39], v[180:183], v[228:231], v[36:39]\n"
      "ds_read_b128 v[204:207], v248 offset:16384\n"
      "v_mfma_f32_16x16x32_bf16 v[40:43], v[184:187], v[212:215], v[40:43]\n"
      "ds_read_b128 v[208:211], v248 offset:18432\n"
      "s_add_u32 m0, s65, 33792\n"
      "s_add_u32 s57, s66, 144000\n"
      "buffer_load_dwordx4 v243, s[44:47], s57 offen lds\n"
      "v_mfma_f32_16x16x32_bf16 v[44:47], v[184:187], v[216:219], v[44:47]\n"
      "v_mfma_f32_16x16x32_bf16 v[48:51], v[184:187], v[220:223], v[48:51]\n"
      "s_add_u32 m0, s65, 34816\n"
      "s_add_u32 s57, s66, 144128\n"
      "buffer_load_dwordx4 v243, s[44:47], s57 offen lds\n"
      "v_mfma_f32_16x16x32_bf16 v[52:55], v[184:187], v[224:227], v[52:55]\n"
      "v_mfma_f32_16x16x32_bf16 v[56:59], v[184:187], v[228:231], v[56:59]\n"
      "s_waitcnt vmcnt(14)\n"
      "s_waitcnt lgkmcnt(0)\n"
      "s_barrier\n"
      "v_mfma_f32_16x16x32_bf16 v[0:3], v[160:163], v[192:195], v[0:3]\n"
      "ds_read_b64_tr_b16 v[176:177], v236 offset:28672\n"
      "s_add_u32 m0, s65, 57344\n"
      "s_add_u32 s57, s66, 172032\n"
      "buffer_load_dwordx4 v243, s[44:47], s57 offen lds\n"
      "v_mfma_f32_16x16x32_bf16 v[4:7], v[160:163], v[196:199], v[4:7]\n"
      "ds_read_b64_tr_b16 v[178:179], v236 offset:29184\n"
      "v_mfma_f32_16x16x32_bf16 v[8:11], v[160:163], v[200:203], v[8:11]\n"
      "ds_read_b64_tr_b16 v[180:181], v237 offset:28672\n"
      "s_add_u32 m0, s65, 58368\n"
      "s_add_u32 s57, s66, 172160\n"
      "buffer_load_dwordx4 v243, s[44:47], s57 offen lds\n"
      "v_mfma_f32_16x16x32_bf16 v[12:15], v[160:163], v[204:207], v[12:15]\n"
      "ds_read_b64_tr_b16 v[182:183], v237 offset:29184\n"
      "v_mfma_f32_16x16x32_bf16 v[16:19], v[160:163], v[208:211], v[16:19]\n"
      "ds_read_b64_tr_b16 v[184:185], v238 offset:28672\n"
      "s_add_u32 m0, s65, 59392\n"
      "s_add_u32 s57, s66, 172288\n"
      "buffer_load_dwordx4 v243, s[44:47], s57 offen lds\n"
      "v_mfma_f32_16x16x32_bf16 v[20:23], v[164:167], v[192:195], v[20:23]\n"
      "ds_read_b64_tr_b16 v[186:187], v238 offset:29184\n"
      "v_mfma_f32_16x16x32_bf16 v[24:27], v[164:167], v[196:199], v[24:27]\n"
      "ds_read_b128 v[212:215], v249 offset:10240\n"
      "s_add_u32 m0, s65, 60416\n"
      "s_add_u32 s57, s66, 172416\n"
      "buffer_load_dwordx4 v243, s[44:47], s57 offen lds\n"
      "v_mfma_f32_16x16x32_bf16 v[28:31], v[164:167], v[200:203], v[28:31]\n"
      "ds_read_b128 v[216:219], v249 offset:12288\n"
      "v_mfma_f32_16x16x32_bf16 v[32:35], v[164:167], v[204:207], v[32:35]\n"
      "ds_read_b128 v[220:223], v249 offset:14336\n"
      "s_add_u32 m0, s65, 61440\n"
      "s_add_u32 s57, s66, 172544\n"
      "buffer_load_dwordx4 v243, s[44:47], s57 offen lds\n"
      "v_mfma_f32_16x16x32_bf16 v[36:39], v[164:167], v[208:211], v[36:39]\n"
      "ds_read_b128 v[224:227], v249 offset:16384\n"
      "v_mfma_f32_16x16x32_bf16 v[40:43], v[168:171], v[192:195], v[40:43]\n"
      "ds_read_b128 v[228:231], v249 offset:18432\n"
      "s_add_u32 m0, s65, 62464\n"
      "s_add_u32 s57, s66, 172672\n"
      "buffer_load_dwordx4 v243, s[44:47], s57 offen lds\n"
      "v_mfma_f32_16x16x32_bf16 v[44:47], v[168:171], v[196:199], v[44:47]\n"
      "v_mfma_f32_16x16x32_bf16 v[48:51], v[168:171], v[200:203], v[48:51]\n"
      "s_add_u32 m0, s65, 63488\n"
      "s_add_u32 s57, s66, 172800\n"
      "buffer_load_dwordx4 v243, s[44:47], s57 offen lds\n"
      "v_mfma_f32_16x16x32_bf16 v[52:55], v[168:171], v[204:207], v[52:55]\n"
      "v_mfma_f32_16x16x32_bf16 v[56:59], v[168:171], v[208:211], v[56:59]\n"
      "s_waitcnt vmcnt(14)\n"
      "s_waitcnt lgkmcnt(0)\n"
      "s_barrier\n"
      "v_mfma_f32_16x16x32_bf16 v[0:3], v[176:179], v[212:215], v[0:3]\n"
      "ds_read_b64_tr_b16 v[160:161], v232 offset:0\n"
      "s_add_u32 m0, s65, 86016\n"
      "s_add_u32 s57, s66, 200704\n"
      "buffer_load_dwordx4 v243, s[44:47], s57 offen lds\n"
      "v_mfma_f32_16x16x32_bf16 v[4:7], v[176:179], v[216:219], v[4:7]\n"
      "ds_read_b64_tr_b16 v[162:163], v232 offset:512\n"
      "v_mfma_f32_16x16x32_bf16 v[8:11], v[176:179], v[220:223], v[8:11]\n"
      "ds_read_b64_tr_b16 v[164:165], v233 offset:0\n"
      "s_add_u32 m0, s65, 87040\n"
      "s_add_u32 s57, s66, 200832\n"
      "buffer_load_dwordx4 v243, s[44:47], s57 offen lds\n"
      "v_mfma_f32_16x16x32_bf16 v[12:15], v[176:179], v[224:227], v[12:15]\n"
      "ds_read_b64_tr_b16 v[166:167], v233 offset:512\n"
      "v_mfma_f32_16x16x32_bf16 v[16:19], v[176:179], v[228:231], v[16:19]\n"
      "ds_read_b64_tr_b16 v[168:169], v234 offset:0\n"
      "s_add_u32 m0, s65, 88064\n"
      "s_add_u32 s57, s66, 200960\n"
      "buffer_load_dwordx4 v243, s[44:47], s57 offen lds\n"
      "v_mfma_f32_16x16x32_bf16 v[20:23], v[180:183], v[212:215], v[20:23]\n"
      "ds_read_b64_tr_b16 v[170:171], v234 offset:512\n"
      "v_mfma_f32_16x16x32_bf16 v[24:27], v[180:183], v[216:219], v[24:27]\n"
      "ds_read_b128 v[192:195], v248 offset:20480\n"
      "s_add_u32 m0, s65, 89088\n"
      "s_add_u32 s57, s66, 201088\n"
      "buffer_load_dwordx4 v243, s[44:47], s57 offen lds\n"
      "v_mfma_f32_16x16x32_bf16 v[28:31], v[180:183], v[220:223], v[28:31]\n"
      "ds_read_b128 v[196:199], v248 offset:22528\n"
      "v_mfma_f32_16x16x32_bf16 v[32:35], v[180:183], v[224:227], v[32:35]\n"
      "ds_read_b128 v[200:203], v248 offset:24576\n"
      "s_add_u32 m0, s65, 90112\n"
      "s_add_u32 s57, s66, 201216\n"
      "buffer_load_dwordx4 v243, s[44:47], s57 offen lds\n"
      "v_mfma_f32_16x16x32_bf16 v[36:39], v[180:183], v[228:231], v[36:39]\n"
      "ds_read_b128 v[204:207], v248 offset:26624\n"
      "v_mfma_f32_16x16x32_bf16 v[40:43], v[184:187], v[212:215], v[40:43]\n"
      "ds_read_b128 v[208:211], v248 offset:28672\n"
      "s_add_u32 m0, s65, 91136\n"
      "s_add_u32 s57, s66, 201344\n"
      "buffer_load_dwordx4 v243, s[44:47], s57 offen lds\n"
      "v_mfma_f32_16x16x32_bf16 v[44:47], v[184:187], v[216:219], v[44:47]\n"
      "v_mfma_f32_16x16x32_bf16 v[48:51], v[184:187], v[220:223], v[48:51]\n"
      "s_add_u32 m0, s65, 92160\n"
      "s_add_u32 s57, s66, 201472\n"
      "buffer_load_dwordx4 v243, s[44:47], s57 offen lds\n"
      "v_mfma_f32_16x16x32_bf16 v[52:55], v[184:187], v[224:227], v[52:55]\n"
      "v_mfma_f32_16x16x32_bf16 v[56:59], v[184:187], v[228:231], v[56:59]\n"
      "s_waitcnt vmcnt(14)\n"
      "s_waitcnt lgkmcnt(0)\n"
      "s_barrier\n"
      "v_mfma_f32_16x16x32_bf16 v[0:3], v[160:163], v[192:195], v[0:3]\n"
      "ds_read_b64_tr_b16 v[176:177], v232 offset:28672\n"
      "v_mfma_f32_16x16x32_bf16 v[4:7], v[160:163], v[196:199], v[4:7]\n"
      "ds_read_b64_tr_b16 v[178:179], v232 offset:29184\n"
      "v_mfma_f32_16x16x32_bf16 v[8:11], v[160:163], v[200:203], v[8:11]\n"
      "ds_read_b64_tr_b16 v[180:181], v233 offset:28672\n"
      "v_mfma_f32_16x16x32_bf16 v[12:15], v[160:163], v[204:207], v[12:15]\n"
      "ds_read_b64_tr_b16 v[182:183], v233 offset:29184\n"
      "v_mfma_f32_16x16x32_bf16 v[16:19], v[160:163], v[208:211], v[16:19]\n"
      "ds_read_b64_tr_b16 v[184:185], v234 offset:28672\n"
      "v_mfma_f32_16x16x32_bf16 v[20:23], v[164:167], v[192:195], v[20:23]\n"
      "ds_read_b64_tr_b16 v[186:187], v234 offset:29184\n"
      "v_mfma_f32_16x16x32_bf16 v[24:27], v[164:167], v[196:199], v[24:27]\n"
      "ds_read_b128 v[212:215], v249 offset:20480\n"
      "v_mfma_f32_16x16x32_bf16 v[28:31], v[164:167], v[200:203], v[28:31]\n"
      "ds_read_b128 v[216:219], v249 offset:22528\n"
      "v_mfma_f32_16x16x32_bf16 v[32:35], v[164:167], v[204:207], v[32:35]\n"
      "ds_read_b128 v[220:223], v249 offset:24576\n"
      "v_mfma_f32_16x16x32_bf16 v[36:39], v[164:167], v[208:211], v[36:39]\n"
      "ds_read_b128 v[224:227], v249 offset:26624\n"
      "v_mfma_f32_16x16x32_bf16 v[40:43], v[168:171], v[192:195], v[40:43]\n"
      "ds_read_b128 v[228:231], v249 offset:28672\n"
      "v_mfma_f32_16x16x32_bf16 v[44:47], v[168:171], v[196:199], v[44:47]\n"
      "v_mfma_f32_16x16x32_bf16 v[48:51], v[168:171], v[200:203], v[48:51]\n"
      "v_mfma_f32_16x16x32_bf16 v[52:55], v[168:171], v[204:207], v[52:55]\n"
      "v_mfma_f32_16x16x32_bf16 v[56:59], v[168:171], v[208:211], v[56:59]\n"
      "s_waitcnt vmcnt(7)\n"
      "s_waitcnt lgkmcnt(0)\n"
      "s_barrier\n"
      "v_mfma_f32_16x16x32_bf16 v[0:3], v[176:179], v[212:215], v[0:3]\n"
      "ds_read_b64_tr_b16 v[160:161], v236 offset:0\n"
      "v_mfma_f32_16x16x32_bf16 v[4:7], v[176:179], v[216:219], v[4:7]\n"
      "ds_read_b64_tr_b16 v[162:163], v236 offset:512\n"
      "v_mfma_f32_16x16x32_bf16 v[8:11], v[176:179], v[220:223], v[8:11]\n"
      "ds_read_b64_tr_b16 v[164:165], v237 offset:0\n"
      "v_mfma_f32_16x16x32_bf16 v[12:15], v[176:179], v[224:227], v[12:15]\n"
      "ds_read_b64_tr_b16 v[166:167], v237 offset:512\n"
      "v_mfma_f32_16x16x32_bf16 v[16:19], v[176:179], v[228:231], v[16:19]\n"
      "ds_read_b64_tr_b16 v[168:169], v238 offset:0\n"
      "v_mfma_f32_16x16x32_bf16 v[20:23], v[180:183], v[212:215], v[20:23]\n"
      "ds_read_b64_tr_b16 v[170:171], v238 offset:512\n"
      "v_mfma_f32_16x16x32_bf16 v[24:27], v[180:183], v[216:219], v[24:27]\n"
      "ds_read_b128 v[192:195], v248 offset:30720\n"
      "v_mfma_f32_16x16x32_bf16 v[28:31], v[180:183], v[220:223], v[28:31]\n"
      "ds_read_b128 v[196:199], v248 offset:32768\n"
      "v_mfma_f32_16x16x32_bf16 v[32:35], v[180:183], v[224:227], v[32:35]\n"
      "ds_read_b128 v[200:203], v248 offset:34816\n"
      "v_mfma_f32_16x16x32_bf16 v[36:39], v[180:183], v[228:231], v[36:39]\n"
      "ds_read_b128 v[204:207], v248 offset:36864\n"
      "v_mfma_f32_16x16x32_bf16 v[40:43], v[184:187], v[212:215], v[40:43]\n"
      "ds_read_b128 v[208:211], v248 offset:38912\n"
      "v_mfma_f32_16x16x32_bf16 v[44:47], v[184:187], v[216:219], v[44:47]\n"
      "v_mfma_f32_16x16x32_bf16 v[48:51], v[184:187], v[220:223], v[48:51]\n"
      "v_mfma_f32_16x16x32_bf16 v[52:55], v[184:187], v[224:227], v[52:55]\n"
      "v_mfma_f32_16x16x32_bf16 v[56:59], v[184:187], v[228:231], v[56:59]\n"
      "s_waitcnt vmcnt(0)\n"
      "s_waitcnt lgkmcnt(0)\n"
      "s_barrier\n"
      "v_mfma_f32_16x16x32_bf16 v[0:3], v[160:163], v[192:195], v[0:3]\n"
      "ds_read_b64_tr_b16 v[176:177], v236 offset:28672\n"
      "v_mfma_f32_16x16x32_bf16 v[4:7], v[160:163], v[196:199], v[4:7]\n"
      "ds_read_b64_tr_b16 v[178:179], v236 offset:29184\n"
      "v_mfma_f32_16x16x32_bf16 v[8:11], v[160:163], v[200:203], v[8:11]\n"
      "ds_read_b64_tr_b16 v[180:181], v237 offset:28672\n"
      "v_mfma_f32_16x16x32_bf16 v[12:15], v[160:163], v[204:207], v[12:15]\n"
      "ds_read_b64_tr_b16 v[182:183], v237 offset:29184\n"
      "v_mfma_f32_16x16x32_bf16 v[16:19], v[160:163], v[208:211], v[16:19]\n"
      "ds_read_b64_tr_b16 v[184:185], v238 offset:28672\n"
      "v_mfma_f32_16x16x32_bf16 v[20:23], v[164:167], v[192:195], v[20:23]\n"
      "ds_read_b64_tr_b16 v[186:187], v238 offset:29184\n"
      "v_mfma_f32_16x16x32_bf16 v[24:27], v[164:167], v[196:199], v[24:27]\n"
      "ds_read_b128 v[212:215], v249 offset:30720\n"
      "v_mfma_f32_16x16x32_bf16 v[28:31], v[164:167], v[200:203], v[28:31]\n"
      "ds_read_b128 v[216:219], v249 offset:32768\n"
      "v_mfma_f32_16x16x32_bf16 v[32:35], v[164:167], v[204:207], v[32:35]\n"
      "ds_read_b128 v[220:223], v249 offset:34816\n"
      "v_mfma_f32_16x16x32_bf16 v[36:39], v[164:167], v[208:211], v[36:39]\n"
      "ds_read_b128 v[224:227], v249 offset:36864\n"
      "v_mfma_f32_16x16x32_bf16 v[40:43], v[168:171], v[192:195], v[40:43]\n"
      "ds_read_b128 v[228:231], v249 offset:38912\n"
      "v_mfma_f32_16x16x32_bf16 v[44:47], v[168:171], v[196:199], v[44:47]\n"
      "v_mfma_f32_16x16x32_bf16 v[48:51], v[168:171], v[200:203], v[48:51]\n"
      "v_mfma_f32_16x16x32_bf16 v[52:55], v[168:171], v[204:207], v[52:55]\n"
      "v_mfma_f32_16x16x32_bf16 v[56:59], v[168:171], v[208:211], v[56:59]\n"
      "s_waitcnt lgkmcnt(0)\n"
      "v_lshlrev_b32 v240, 2, v253\n"
      "global_load_dword %[cs], v240, %[chal] sc1\n"
      "v_mfma_f32_16x16x32_bf16 v[0:3], v[176:179], v[212:215], v[0:3]\n"
      "v_mfma_f32_16x16x32_bf16 v[4:7], v[176:179], v[216:219], v[4:7]\n"
      "v_mfma_f32_16x16x32_bf16 v[8:11], v[176:179], v[220:223], v[8:11]\n"
      "v_mfma_f32_16x16x32_bf16 v[12:15], v[176:179], v[224:227], v[12:15]\n"
      "v_mfma_f32_16x16x32_bf16 v[16:19], v[176:179], v[228:231], v[16:19]\n"
      "v_mfma_f32_16x16x32_bf16 v[20:23], v[180:183], v[212:215], v[20:23]\n"
      "v_mfma_f32_16x16x32_bf16 v[24:27], v[180:183], v[216:219], v[24:27]\n"
      "v_mfma_f32_16x16x32_bf16 v[28:31], v[180:183], v[220:223], v[28:31]\n"
      "v_mfma_f32_16x16x32_bf16 v[32:35], v[180:183], v[224:227], v[32:35]\n"
      "v_mfma_f32_16x16x32_bf16 v[36:39], v[180:183], v[228:231], v[36:39]\n"
      "v_mfma_f32_16x16x32_bf16 v[40:43], v[184:187], v[212:215], v[40:43]\n"
      "v_mfma_f32_16x16x32_bf16 v[44:47], v[184:187], v[216:219], v[44:47]\n"
      "v_mfma_f32_16x16x32_bf16 v[48:51], v[184:187], v[220:223], v[48:51]\n"
      "v_mfma_f32_16x16x32_bf16 v[52:55], v[184:187], v[224:227], v[52:55]\n"
      "v_mfma_f32_16x16x32_bf16 v[56:59], v[184:187], v[228:231], v[56:59]\n"
      "s_mov_b64 s[40:41], %[ppart]\n"
      "s_and_b32 s41, s41, 0xffff\n"
      "s_mov_b32 s42, 71680\n"
      "s_mov_b32 s43, 0x00020000\n"
      "v_lshlrev_b32 v248, 4, v251\n"
      "v_lshlrev_b32 v249, 3, v251\n"
      "s_mul_i32 s57, s52, 17920\n"
      "s_nop 4\n"
      "v_cvt_pk_bf16_f32 v160, v0, v1\n"
      "v_cvt_pk_bf16_f32 v161, v2, v3\n"
      "v_cvt_pk_bf16_f32 v162, v20, v21\n"
      "v_cvt_pk_bf16_f32 v163, v22, v23\n"
      "buffer_store_dwordx4 v[160:163], v248, s[40:43], s57 offen offset:2048 sc1\n"
      "v_cvt_pk_bf16_f32 v164, v40, v41\n"
      "v_cvt_pk_bf16_f32 v165, v42, v43\n"
      "buffer_store_dwordx2 v[164:165], v249, s[40:43], s57 offen offset:3072 sc1\n"
      "s_add_u32 s57, s57, 3584\n"
      "v_cvt_pk_bf16_f32 v168, v4, v5\n"
      "v_cvt_pk_bf16_f32 v169, v6, v7\n"
      "v_cvt_pk_bf16_f32 v170, v24, v25\n"
      "v_cvt_pk_bf16_f32 v171, v26, v27\n"
      "buffer_store_dwordx4 v[168:171], v248, s[40:43], s57 offen offset:2048 sc1\n"
      "v_cvt_pk_bf16_f32 v172, v44, v45\n"
      "v_cvt_pk_bf16_f32 v173, v46, v47\n"
      "buffer_store_dwordx2 v[172:173], v249, s[40:43], s57 offen offset:3072 sc1\n"
      "s_add_u32 s57, s57, 3584\n"
      "v_cvt_pk_bf16_f32 v176, v8, v9\n"
      "v_cvt_pk_bf16_f32 v177, v10, v11\n"
      "v_cvt_pk_bf16_f32 v178, v28, v29\n"
      "v_cvt_pk_bf16_f32 v179, v30, v31\n"
      "buffer_store_dwordx4 v[176:179], v248, s[40:43], s57 offen offset:2048 sc1\n"
      "v_cvt_pk_bf16_f32 v180, v48, v49\n"
      "v_cvt_pk_bf16_f32 v181, v50, v51\n"
      "buffer_store_dwordx2 v[180:181], v249, s[40:43], s57 offen offset:3072 sc1\n"
      "s_add_u32 s57, s57, 3584\n"
      "v_cvt_pk_bf16_f32 v184, v12, v13\n"
      "v_cvt_pk_bf16_f32 v185, v14, v15\n"
      "v_cvt_pk_bf16_f32 v186, v32, v33\n"
      "v_cvt_pk_bf16_f32 v187, v34, v35\n"
      "buffer_store_dwordx4 v[184:187], v248, s[40:43], s57 offen offset:2048 sc1\n"
      "v_cvt_pk_bf16_f32 v188, v52, v53\n"
      "v_cvt_pk_bf16_f32 v189, v54, v55\n"
      "buffer_store_dwordx2 v[188:189], v249, s[40:43], s57 offen offset:3072 sc1\n"
      "s_add_u32 s57, s57, 3584\n"
      "s_mov_b32 exec_lo, 0x00ff00ff\n"
      "s_mov_b32 exec_hi, 0x00ff00ff\n"
      "v_cvt_pk_bf16_f32 v160, v16, v17\n"
      "v_cvt_pk_bf16_f32 v161, v18, v19\n"
      "v_cvt_pk_bf16_f32 v162, v36, v37\n"
      "v_cvt_pk_bf16_f32 v163, v38, v39\n"
      "buffer_store_dwordx4 v[160:163], v248, s[40:43], s57 offen offset:2048 sc1\n"
      "v_cvt_pk_bf16_f32 v164, v56, v57\n"
      "v_cvt_pk_bf16_f32 v165, v58, v59\n"
      "buffer_store_dwordx2 v[164:165], v249, s[40:43], s57 offen offset:3072 sc1\n"
      "s_add_u32 s57, s57, 3584\n"
      "s_mov_b64 exec, -1\n"
      "s_branch LEND%=\n"
      "LQKR%=:\n"
      "s_setprio 1\n"
      "v_bfe_u32 v232, v251, 3, 1\n"
      "v_and_b32 v255, 1, v252\n"
      "v_lshl_or_b32 v232, v255, 1, v232\n"
      "v_bfe_u32 v255, v251, 1, 1\n"
      "v_lshl_or_b32 v232, v232, 1, v255\n"
      "v_lshlrev_b32 v232, 4, v232\n"
      "v_and_b32 v255, 1, v251\n"
      "v_lshl_or_b32 v232, v255, 3, v232\n"
      "v_bfe_u32 v255, v251, 2, 2\n"
      "v_lshl_or_b32 v232, v255, 7, v232\n"
      "v_mul_u32_u24 v255, 4096, v252\n"
      "v_add_u32 v232, v255, v232\n"
      "s_lshl_b32 s57, s52, 10\n"
      "v_add_u32 v232, s57, v232\n"
      "v_xor_b32 v233, 32, v232\n"
      "v_xor_b32 v234, 64, v232\n"
      "v_xor_b32 v235, 96, v232\n"
      "v_add_u32 v236, 0x10000, v232\n"
      "v_add_u32 v237, 0x10000, v233\n"
      "v_add_u32 v238, 0x10000, v234\n"
      "v_add_u32 v239, 0x10000, v235\n"
      "v_xor_b32 v248, v252, v254\n"
      "v_lshlrev_b32 v248, 4, v248\n"
      "v_lshl_add_u32 v248, v253, 7, v248\n"
      "v_add_u32 v248, 98304, v248\n"
      "v_xor_b32 v249, 64, v248\n"
      "s_mov_b64 s[36:37], %[pk]\n"
      "s_and_b32 s37, s37, 0xffff\n"
      "s_mov_b32 s38, 524288\n"
      "s_mov_b32 s39, 0x00020000\n"
      "v_lshrrev_b32 v255, 3, v251\n"
      "v_xor_b32 v250, v255, v254\n"
      "v_lshlrev_b32 v250, 4, v250\n"
      "v_mul_u32_u24 v255, 512, v255\n"
      "v_add_u32 v250, v255, v250\n"
      "s_lshl_b32 s55, s52, 13\n"
      "s_mul_i32 s58, s52, 10240\n"
      "s_add_u32 s58, s58, 120320\n"
      "v_lshrrev_b32 v244, 1, v252\n"
      "v_xor_b32 v244, v244, v254\n"
      "v_lshlrev_b32 v244, 4, v244\n"
      "v_and_b32 v255, 1, v252\n"
      "v_lshl_or_b32 v244, v255, 3, v244\n"
      "v_lshl_add_u32 v244, v253, 7, v244\n"
      "v_add_u32 v244, s58, v244\n"
      "v_xor_b32 v245, 32, v244\n"
      "v_xor_b32 v246, 64, v244\n"
      "v_xor_b32 v247, 96, v244\n"
      "s_mov_b32 s98, 0x3db8aa3b\n"
      "s_lshr_b32 s57, s52, 1\n"
      "s_add_u32 s57, s57, %[sub]\n"
      "s_and_b32 s58, s52, 1\n"
      "s_lshl_b32 s58, s58, 13\n"
      "s_add_u32 s94, s57, 0\n"
      "s_and_b32 s94, s94, 3\n"
      "s_lshl_b32 s94, s94, 14\n"
      "s_add_u32 s94, s94, s58\n"
      "s_add_u32 s95, s57, 2\n"
      "s_and_b32 s95, s95, 3\n"
      "s_lshl_b32 s95, s95, 14\n"
      "s_add_u32 s95, s95, s58\n"
      "s_add_u32 s94, s94, 65536\n"
      "s_waitcnt lgkmcnt(0)\n"
      "s_barrier\n"
      "ds_read_b64_tr_b16 v[160:161], v236 offset:0\n"
      "ds_read_b64_tr_b16 v[162:163], v236 offset:512\n"
      "ds_read_b64_tr_b16 v[164:165], v237 offset:0\n"
      "ds_read_b64_tr_b16 v[166:167], v237 offset:512\n"
      "ds_read_b64_tr_b16 v[168:169], v238 offset:0\n"
      "ds_read_b64_tr_b16 v[170:171], v238 offset:512\n"
      "ds_read_b64_tr_b16 v[172:173], v239 offset:0\n"
      "ds_read_b64_tr_b16 v[174:175], v239 offset:512\n"
      "ds_read_b128 v[192:195], v248 offset:0\n"
      "ds_read_b128 v[196:199], v248 offset:2048\n"
      "ds_read_b128 v[200:203], v248 offset:4096\n"
      "ds_read_b128 v[204:207], v248 offset:6144\n"
      "ds_read_b128 v[208:211], v248 offset:8192\n"
      "s_waitcnt lgkmcnt(0)\n"
      "v_mfma_f32_16x16x32_bf16 v[0:3], v[160:163], v[192:195], 0\n"
      "ds_read_b64_tr_b16 v[176:177], v236 offset:16384\n"
      "v_mfma_f32_16x16x32_bf16 v[4:7], v[160:163], v[196:199], 0\n"
      "ds_read_b64_tr_b16 v[178:179], v236 offset:16896\n"
      "v_mfma_f32_16x16x32_bf16 v[8:11], v[160:163], v[200:203], 0\n"
      "ds_read_b64_tr_b16 v[180:181], v237 offset:16384\n"
      "v_mfma_f32_16x16x32_bf16 v[12:15], v[160:163], v[204:207], 0\n"
      "ds_read_b64_tr_b16 v[182:183], v237 offset:16896\n"
      "v_mfma_f32_16x16x32_bf16 v[16:19], v[160:163], v[208:211], 0\n"
      "ds_read_b64_tr_b16 v[184:185], v238 offset:16384\n"
      "v_mfma_f32_16x16x32_bf16 v[20:23], v[164:167], v[192:195], 0\n"
      "ds_read_b64_tr_b16 v[186:187], v238 offset:16896\n"
      "v_mfma_f32_16x16x32_bf16 v[24:27], v[164:167], v[196:199], 0\n"
      "ds_read_b64_tr_b16 v[188:189], v239 offset:16384\n"
      "v_mfma_f32_16x16x32_bf16 v[28:31], v[164:167], v[200:203], 0\n"
      "v_mfma_f32_16x16x32_bf16 v[32:35], v[164:167], v[204:207], 0\n"
      "ds_read_b64_tr_b16 v[190:191], v239 offset:16896\n"
      "v_mfma_f32_16x16x32_bf16 v[36:39], v[164:167], v[208:211], 0\n"
      "ds_read_b128 v[212:215], v249 offset:0\n"
      "v_mfma_f32_16x16x32_bf16 v[40:43], v[168:171], v[192:195], 0\n"
      "ds_read_b128 v[216:219], v249 offset:2048\n"
      "v_mfma_f32_16x16x32_bf16 v[44:47], v[168:171], v[196:199], 0\n"
      "ds_read_b128 v[220:223], v249 offset:4096\n"
      "v_mfma_f32_16x16x32_bf16 v[48:51], v[168:171], v[200:203], 0\n"
      "ds_read_b128 v[224:227], v249 offset:6144\n"
      "v_mfma_f32_16x16x32_bf16 v[52:55], v[168:171], v[204:207], 0\n"
      "ds_read_b128 v[228:231], v249 offset:8192\n"
      "v_mfma_f32_16x16x32_bf16 v[56:59], v[168:171], v[208:211], 0\n"
      "v_mfma_f32_16x16x32_bf16 v[60:63], v[172:175], v[192:195], 0\n"
      "v_mfma_f32_16x16x32_bf16 v[64:67], v[172:175], v[196:199], 0\n"
      "v_mfma_f32_16x16x32_bf16 v[68:71], v[172:175], v[200:203], 0\n"
      "v_mfma_f32_16x16x32_bf16 v[72:75], v[172:175], v[204:207], 0\n"
      "v_mfma_f32_16x16x32_bf16 v[76:79], v[172:175], v[208:211], 0\n"
      "s_waitcnt lgkmcnt(0)\n"
      "v_mfma_f32_16x16x32_bf16 v[0:3], v[176:179], v[212:215], v[0:3]\n"
      "ds_read_b64_tr_b16 v[160:161], v232 offset:0\n"
      "v_mfma_f32_16x16x32_bf16 v[4:7], v[176:179], v[216:219], v[4:7]\n"
      "ds_read_b64_tr_b16 v[162:163], v232 offset:512\n"
      "v_mfma_f32_16x16x32_bf16 v[8:11], v[176:179], v[220:223], v[8:11]\n"
      "ds_read_b64_tr_b16 v[164:165], v233 offset:0\n"
      "v_mfma_f32_16x16x32_bf16 v[12:15], v[176:179], v[224:227], v[12:15]\n"
      "ds_read_b64_tr_b16 v[166:167], v233 offset:512\n"
      "v_mfma_f32_16x16x32_bf16 v[16:19], v[176:179], v[228:231], v[16:19]\n"
      "ds_read_b64_tr_b16 v[168:169], v234 offset:0\n"
      "v_mfma_f32_16x16x32_bf16 v[20:23], v[180:183], v[212:215], v[20:23]\n"
      "ds_read_b64_tr_b16 v[170:171], v234 offset:512\n"
      "v_mfma_f32_16x16x32_bf16 v[24:27], v[180:183], v[216:219], v[24:27]\n"
      "ds_read_b64_tr_b16 v[172:173], v235 offset:0\n"
      "v_mfma_f32_16x16x32_bf16 v[28:31], v[180:183], v[220:223], v[28:31]\n"
      "v_mfma_f32_16x16x32_bf16 v[32:35], v[180:183], v[224:227], v[32:35]\n"
      "ds_read_b64_tr_b16 v[174:175], v235 offset:512\n"
      "v_mfma_f32_16x16x32_bf16 v[36:39], v[180:183], v[228:231], v[36:39]\n"
      "ds_read_b128 v[192:195], v248 offset:9216\n"
      "v_mfma_f32_16x16x32_bf16 v[40:43], v[184:187], v[212:215], v[40:43]\n"
      "ds_read_b128 v[196:199], v248 offset:11264\n"
      "v_mfma_f32_16x16x32_bf16 v[44:47], v[184:187], v[216:219], v[44:47]\n"
      "ds_read_b128 v[200:203], v248 offset:13312\n"
      "v_mfma_f32_16x16x32_bf16 v[48:51], v[184:187], v[220:223], v[48:51]\n"
      "ds_read_b128 v[204:207], v248 offset:15360\n"
      "v_mfma_f32_16x16x32_bf16 v[52:55], v[184:187], v[224:227], v[52:55]\n"
      "ds_read_b128 v[208:211], v248 offset:17408\n"
      "v_mfma_f32_16x16x32_bf16 v[56:59], v[184:187], v[228:231], v[56:59]\n"
      "v_mfma_f32_16x16x32_bf16 v[60:63], v[188:191], v[212:215], v[60:63]\n"
      "v_mfma_f32_16x16x32_bf16 v[64:67], v[188:191], v[216:219], v[64:67]\n"
      "v_mfma_f32_16x16x32_bf16 v[68:71], v[188:191], v[220:223], v[68:71]\n"
      "v_mfma_f32_16x16x32_bf16 v[72:75], v[188:191], v[224:227], v[72:75]\n"
      "v_mfma_f32_16x16x32_bf16 v[76:79], v[188:191], v[228:231], v[76:79]\n"
      "s_waitcnt lgkmcnt(0)\n"
      "s_barrier\n"
      "s_waitcnt lgkmcnt(0)\n"
      "v_mfma_f32_16x16x32_bf16 v[0:3], v[160:163], v[192:195], v[0:3]\n"
      "ds_read_b64_tr_b16 v[176:177], v232 offset:16384\n"
      "v_mfma_f32_16x16x32_bf16 v[4:7], v[160:163], v[196:199], v[4:7]\n"
      "ds_read_b64_tr_b16 v[178:179], v232 offset:16896\n"
      "v_mfma_f32_16x16x32_bf16 v[8:11], v[160:163], v[200:203], v[8:11]\n"
      "ds_read_b64_tr_b16 v[180:181], v233 offset:16384\n"
      "v_mfma_f32_16x16x32_bf16 v[12:15], v[160:163], v[204:207], v[12:15]\n"
      "ds_read_b64_tr_b16 v[182:183], v233 offset:16896\n"
      "v_mfma_f32_16x16x32_bf16 v[16:19], v[160:163], v[208:211], v[16:19]\n"
      "ds_read_b64_tr_b16 v[184:185], v234 offset:16384\n"
      "v_mfma_f32_16x16x32_bf16 v[20:23], v[164:167], v[192:195], v[20:23]\n"
      "ds_read_b64_tr_b16 v[186:187], v234 offset:16896\n"
      "v_mfma_f32_16x16x32_bf16 v[24:27], v[164:167], v[196:199], v[24:27]\n"
      "ds_read_b64_tr_b16 v[188:189], v235 offset:16384\n"
      "v_mfma_f32_16x16x32_bf16 v[28:31], v[164:167], v[200:203], v[28:31]\n"
      "v_mfma_f32_16x16x32_bf16 v[32:35], v[164:167], v[204:207], v[32:35]\n"
      "ds_read_b64_tr_b16 v[190:191], v235 offset:16896\n"
      "v_mfma_f32_16x16x32_bf16 v[36:39], v[164:167], v[208:211], v[36:39]\n"
      "ds_read_b128 v[212:215], v249 offset:9216\n"
      "v_mfma_f32_16x16x32_bf16 v[40:43], v[168:171], v[192:195], v[40:43]\n"
      "ds_read_b128 v[216:219], v249 offset:11264\n"
      "v_mfma_f32_16x16x32_bf16 v[44:47], v[168:171], v[196:199], v[44:47]\n"
      "ds_read_b128 v[220:223], v249 offset:13312\n"
      "v_mfma_f32_16x16x32_bf16 v[48:51], v[168:171], v[200:203], v[48:51]\n"
      "ds_read_b128 v[224:227], v249 offset:15360\n"
      "v_mfma_f32_16x16x32_bf16 v[52:55], v[168:171], v[204:207], v[52:55]\n"
      "ds_read_b128 v[228:231], v249 offset:17408\n"
      "v_mfma_f32_16x16x32_bf16 v[56:59], v[168:171], v[208:211], v[56:59]\n"
      "v_mfma_f32_16x16x32_bf16 v[60:63], v[172:175], v[192:195], v[60:63]\n"
      "v_mfma_f32_16x16x32_bf16 v[64:67], v[172:175], v[196:199], v[64:67]\n"
      "v_mfma_f32_16x16x32_bf16 v[68:71], v[172:175], v[200:203], v[68:71]\n"
      "v_mfma_f32_16x16x32_bf16 v[72:75], v[172:175], v[204:207], v[72:75]\n"
      "v_mfma_f32_16x16x32_bf16 v[76:79], v[172:175], v[208:211], v[76:79]\n"
      "s_waitcnt lgkmcnt(0)\n"
      "v_mfma_f32_16x16x32_bf16 v[0:3], v[176:179], v[212:215], v[0:3]\n"
      "ds_read_b64_tr_b16 v[160:161], v232 offset:32768\n"
      "v_mfma_f32_16x16x32_bf16 v[4:7], v[176:179], v[216:219], v[4:7]\n"
      "ds_read_b64_tr_b16 v[162:163], v232 offset:33280\n"
      "v_mfma_f32_16x16x32_bf16 v[8:11], v[176:179], v[220:223], v[8:11]\n"
      "ds_read_b64_tr_b16 v[164:165], v233 offset:32768\n"
      "v_mfma_f32_16x16x32_bf16 v[12:15], v[176:179], v[224:227], v[12:15]\n"
      "ds_read_b64_tr_b16 v[166:167], v233 offset:33280\n"
      "v_mfma_f32_16x16x32_bf16 v[16:19], v[176:179], v[228:231], v[16:19]\n"
      "ds_read_b64_tr_b16 v[168:169], v234 offset:32768\n"
      "v_mfma_f32_16x16x32_bf16 v[20:23], v[180:183], v[212:215], v[20:23]\n"
      "ds_read_b64_tr_b16 v[170:171], v234 offset:33280\n"
      "v_mfma_f32_16x16x32_bf16 v[24:27], v[180:183], v[216:219], v[24:27]\n"
      "ds_read_b64_tr_b16 v[172:173], v235 offset:32768\n"
      "v_mfma_f32_16x16x32_bf16 v[28:31], v[180:183], v[220:223], v[28:31]\n"
      "v_mfma_f32_16x16x32_bf16 v[32:35], v[180:183], v[224:227], v[32:35]\n"
      "ds_read_b64_tr_b16 v[174:175], v235 offset:33280\n"
      "v_mfma_f32_16x16x32_bf16 v[36:39], v[180:183], v[228:231], v[36:39]\n"
      "ds_read_b128 v[192:195], v248 offset:18432\n"
      "v_mfma_f32_16x16x32_bf16 v[40:43], v[184:187], v[212:215], v[40:43]\n"
      "ds_read_b128 v[196:199], v248 offset:20480\n"
      "v_mfma_f32_16x16x32_bf16 v[44:47], v[184:187], v[216:219], v[44:47]\n"
      "ds_read_b128 v[200:203], v248 offset:22528\n"
      "v_mfma_f32_16x16x32_bf16 v[48:51], v[184:187], v[220:223], v[48:51]\n"
      "ds_read_b128 v[204:207], v248 offset:24576\n"
      "v_mfma_f32_16x16x32_bf16 v[52:55], v[184:187], v[224:227], v[52:55]\n"
      "ds_read_b128 v[208:211], v248 offset:26624\n"
      "v_mfma_f32_16x16x32_bf16 v[56:59], v[184:187], v[228:231], v[56:59]\n"
      "v_mfma_f32_16x16x32_bf16 v[60:63], v[188:191], v[212:215], v[60:63]\n"
      "v_mfma_f32_16x16x32_bf16 v[64:67], v[188:191], v[216:219], v[64:67]\n"
      "v_mfma_f32_16x16x32_bf16 v[68:71], v[188:191], v[220:223], v[68:71]\n"
      "v_mfma_f32_16x16x32_bf16 v[72:75], v[188:191], v[224:227], v[72:75]\n"
      "v_mfma_f32_16x16x32_bf16 v[76:79], v[188:191], v[228:231], v[76:79]\n"
      "s_waitcnt lgkmcnt(0)\n"
      "s_barrier\n"
      "s_waitcnt lgkmcnt(0)\n"
      "v_mfma_f32_16x16x32_bf16 v[0:3], v[160:163], v[192:195], v[0:3]\n"
      "ds_read_b64_tr_b16 v[176:177], v232 offset:49152\n"
      "v_mfma_f32_16x16x32_bf16 v[4:7], v[160:163], v[196:199], v[4:7]\n"
      "ds_read_b64_tr_b16 v[178:179], v232 offset:49664\n"
      "v_mfma_f32_16x16x32_bf16 v[8:11], v[160:163], v[200:203], v[8:11]\n"
      "ds_read_b64_tr_b16 v[180:181], v233 offset:49152\n"
      "v_mfma_f32_16x16x32_bf16 v[12:15], v[160:163], v[204:207], v[12:15]\n"
      "ds_read_b64_tr_b16 v[182:183], v233 offset:49664\n"
      "v_mfma_f32_16x16x32_bf16 v[16:19], v[160:163], v[208:211], v[16:19]\n"
      "ds_read_b64_tr_b16 v[184:185], v234 offset:49152\n"
      "v_mfma_f32_16x16x32_bf16 v[20:23], v[164:167], v[192:195], v[20:23]\n"
      "ds_read_b64_tr_b16 v[186:187], v234 offset:49664\n"
      "v_mfma_f32_16x16x32_bf16 v[24:27], v[164:167], v[196:199], v[24:27]\n"
      "ds_read_b64_tr_b16 v[188:189], v235 offset:49152\n"
      "v_mfma_f32_16x16x32_bf16 v[28:31], v[164:167], v[200:203], v[28:31]\n"
      "v_mfma_f32_16x16x32_bf16 v[32:35], v[164:167], v[204:207], v[32:35]\n"
      "ds_read_b64_tr_b16 v[190:191], v235 offset:49664\n"
      "v_mfma_f32_16x16x32_bf16 v[36:39], v[164:167], v[208:211], v[36:39]\n"
      "ds_read_b128 v[212:215], v249 offset:18432\n"
      "v_mfma_f32_16x16x32_bf16 v[40:43], v[168:171], v[192:195], v[40:43]\n"
      "ds_read_b128 v[216:219], v249 offset:20480\n"
      "v_mfma_f32_16x16x32_bf16 v[44:47], v[168:171], v[196:199], v[44:47]\n"
      "ds_read_b128 v[220:223], v249 offset:22528\n"
      "v_mfma_f32_16x16x32_bf16 v[48:51], v[168:171], v[200:203], v[48:51]\n"
      "ds_read_b128 v[224:227], v249 offset:24576\n"
      "v_mfma_f32_16x16x32_bf16 v[52:55], v[168:171], v[204:207], v[52:55]\n"
      "ds_read_b128 v[228:231], v249 offset:26624\n"
      "v_mfma_f32_16x16x32_bf16 v[56:59], v[168:171], v[208:211], v[56:59]\n"
      "v_mfma_f32_16x16x32_bf16 v[60:63], v[172:175], v[192:195], v[60:63]\n"
      "v_mfma_f32_16x16x32_bf16 v[64:67], v[172:175], v[196:199], v[64:67]\n"
      "v_mfma_f32_16x16x32_bf16 v[68:71], v[172:175], v[200:203], v[68:71]\n"
      "v_mfma_f32_16x16x32_bf16 v[72:75], v[172:175], v[204:207], v[72:75]\n"
      "v_mfma_f32_16x16x32_bf16 v[76:79], v[172:175], v[208:211], v[76:79]\n"
      "s_waitcnt lgkmcnt(0)\n"
      "v_mfma_f32_16x16x32_bf16 v[0:3], v[176:179], v[212:215], v[0:3]\n"
      "ds_read_b64_tr_b16 v[160:161], v236 offset:0\n"
      "v_mfma_f32_16x16x32_bf16 v[4:7], v[176:179], v[216:219], v[4:7]\n"
      "ds_read_b64_tr_b16 v[162:163], v236 offset:512\n"
      "v_mfma_f32_16x16x32_bf16 v[8:11], v[176:179], v[220:223], v[8:11]\n"
      "ds_read_b64_tr_b16 v[164:165], v237 offset:0\n"
      "v_mfma_f32_16x16x32_bf16 v[12:15], v[176:179], v[224:227], v[12:15]\n"
      "ds_read_b64_tr_b16 v[166:167], v237 offset:512\n"
      "v_mfma_f32_16x16x32_bf16 v[16:19], v[176:179], v[228:231], v[16:19]\n"
      "ds_read_b64_tr_b16 v[168:169], v238 offset:0\n"
      "v_mfma_f32_16x16x32_bf16 v[20:23], v[180:183], v[212:215], v[20:23]\n"
      "ds_read_b64_tr_b16 v[170:171], v238 offset:512\n"
      "v_mfma_f32_16x16x32_bf16 v[24:27], v[180:183], v[216:219], v[24:27]\n"
      "ds_read_b64_tr_b16 v[172:173], v239 offset:0\n"
      "v_mfma_f32_16x16x32_bf16 v[28:31], v[180:183], v[220:223], v[28:31]\n"
      "v_mfma_f32_16x16x32_bf16 v[32:35], v[180:183], v[224:227], v[32:35]\n"
      "ds_read_b64_tr_b16 v[174:175], v239 offset:512\n"
      "v_mfma_f32_16x16x32_bf16 v[36:39], v[180:183], v[228:231], v[36:39]\n"
      "ds_read_b128 v[192:195], v248 offset:27648\n"
      "v_mfma_f32_16x16x32_bf16 v[40:43], v[184:187], v[212:215], v[40:43]\n"
      "ds_read_b128 v[196:199], v248 offset:29696\n"
      "v_mfma_f32_16x16x32_bf16 v[44:47], v[184:187], v[216:219], v[44:47]\n"
      "ds_read_b128 v[200:203], v248 offset:31744\n"
      "v_mfma_f32_16x16x32_bf16 v[48:51], v[184:187], v[220:223], v[48:51]\n"
      "ds_read_b128 v[204:207], v248 offset:33792\n"
      "v_mfma_f32_16x16x32_bf16 v[52:55], v[184:187], v[224:227], v[52:55]\n"
      "ds_read_b128 v[208:211], v248 offset:35840\n"
      "v_mfma_f32_16x16x32_bf16 v[56:59], v[184:187], v[228:231], v[56:59]\n"
      "v_mfma_f32_16x16x32_bf16 v[60:63], v[188:191], v[212:215], v[60:63]\n"
      "v_mfma_f32_16x16x32_bf16 v[64:67], v[188:191], v[216:219], v[64:67]\n"
      "v_mfma_f32_16x16x32_bf16 v[68:71], v[188:191], v[220:223], v[68:71]\n"
      "v_mfma_f32_16x16x32_bf16 v[72:75], v[188:191], v[224:227], v[72:75]\n"
      "v_mfma_f32_16x16x32_bf16 v[76:79], v[188:191], v[228:231], v[76:79]\n"
      "s_waitcnt lgkmcnt(0)\n"
      "s_barrier\n"
      "s_waitcnt lgkmcnt(0)\n"
      "v_mfma_f32_16x16x32_bf16 v[0:3], v[160:163], v[192:195], v[0:3]\n"
      "ds_read_b64_tr_b16 v[176:177], v236 offset:16384\n"
      "v_mfma_f32_16x16x32_bf16 v[4:7], v[160:163], v[196:199], v[4:7]\n"
      "ds_read_b64_tr_b16 v[178:179], v236 offset:16896\n"
      "v_mfma_f32_16x16x32_bf16 v[8:11], v[160:163], v[200:203], v[8:11]\n"
      "ds_read_b64_tr_b16 v[180:181], v237 offset:16384\n"
      "v_mfma_f32_16x16x32_bf16 v[12:15], v[160:163], v[204:207], v[12:15]\n"
      "ds_read_b64_tr_b16 v[182:183], v237 offset:16896\n"
      "v_mfma_f32_16x16x32_bf16 v[16:19], v[160:163], v[208:211], v[16:19]\n"
      "ds_read_b64_tr_b16 v[184:185], v238 offset:16384\n"
      "v_mfma_f32_16x16x32_bf16 v[20:23], v[164:167], v[192:195], v[20:23]\n"
      "ds_read_b64_tr_b16 v[186:187], v238 offset:16896\n"
      "v_mfma_f32_16x16x32_bf16 v[24:27], v[164:167], v[196:199], v[24:27]\n"
      "ds_read_b64_tr_b16 v[188:189], v239 offset:16384\n"
      "v_mfma_f32_16x16x32_bf16 v[28:31], v[164:167], v[200:203], v[28:31]\n"
      "v_mfma_f32_16x16x32_bf16 v[32:35], v[164:167], v[204:207], v[32:35]\n"
      "ds_read_b64_tr_b16 v[190:191], v239 offset:16896\n"
      "v_mfma_f32_16x16x32_bf16 v[36:39], v[164:167], v[208:211], v[36:39]\n"
      "ds_read_b128 v[212:215], v249 offset:27648\n"
      "v_mfma_f32_16x16x32_bf16 v[40:43], v[168:171], v[192:195], v[40:43]\n"
      "ds_read_b128 v[216:219], v249 offset:29696\n"
      "v_mfma_f32_16x16x32_bf16 v[44:47], v[168:171], v[196:199], v[44:47]\n"
      "ds_read_b128 v[220:223], v249 offset:31744\n"
      "v_mfma_f32_16x16x32_bf16 v[48:51], v[168:171], v[200:203], v[48:51]\n"
      "ds_read_b128 v[224:227], v249 offset:33792\n"
      "v_mfma_f32_16x16x32_bf16 v[52:55], v[168:171], v[204:207], v[52:55]\n"
      "ds_read_b128 v[228:231], v249 offset:35840\n"
      "v_mfma_f32_16x16x32_bf16 v[56:59], v[168:171], v[208:211], v[56:59]\n"
      "v_mfma_f32_16x16x32_bf16 v[60:63], v[172:175], v[192:195], v[60:63]\n"
      "v_mfma_f32_16x16x32_bf16 v[64:67], v[172:175], v[196:199], v[64:67]\n"
      "v_mfma_f32_16x16x32_bf16 v[68:71], v[172:175], v[200:203], v[68:71]\n"
      "v_mfma_f32_16x16x32_bf16 v[72:75], v[172:175], v[204:207], v[72:75]\n"
      "v_mfma_f32_16x16x32_bf16 v[76:79], v[172:175], v[208:211], v[76:79]\n"
      "s_waitcnt lgkmcnt(0)\n"
      "v_mfma_f32_16x16x32_bf16 v[0:3], v[176:179], v[212:215], v[0:3]\n"
      "ds_read_b64_tr_b16 v[160:161], v232 offset:0\n"
      "v_mfma_f32_16x16x32_bf16 v[4:7], v[176:179], v[216:219], v[4:7]\n"
      "ds_read_b64_tr_b16 v[162:163], v232 offset:512\n"
      "v_mfma_f32_16x16x32_bf16 v[8:11], v[176:179], v[220:223], v[8:11]\n"
      "ds_read_b64_tr_b16 v[164:165], v233 offset:0\n"
      "v_mfma_f32_16x16x32_bf16 v[12:15], v[176:179], v[224:227], v[12:15]\n"
      "ds_read_b64_tr_b16 v[166:167], v233 offset:512\n"
      "v_mfma_f32_16x16x32_bf16 v[16:19], v[176:179], v[228:231], v[16:19]\n"
      "ds_read_b64_tr_b16 v[168:169], v234 offset:0\n"
      "v_mfma_f32_16x16x32_bf16 v[20:23], v[180:183], v[212:215], v[20:23]\n"
      "ds_read_b64_tr_b16 v[170:171], v234 offset:512\n"
      "v_mfma_f32_16x16x32_bf16 v[24:27], v[180:183], v[216:219], v[24:27]\n"
      "ds_read_b64_tr_b16 v[172:173], v235 offset:0\n"
      "v_mfma_f32_16x16x32_bf16 v[28:31], v[180:183], v[220:223], v[28:31]\n"
      "v_mfma_f32_16x16x32_bf16 v[32:35], v[180:183], v[224:227], v[32:35]\n"
      "ds_read_b64_tr_b16 v[174:175], v235 offset:512\n"
      "v_mfma_f32_16x16x32_bf16 v[36:39], v[180:183], v[228:231], v[36:39]\n"
      "ds_read_b128 v[192:195], v248 offset:36864\n"
      "v_mfma_f32_16x16x32_bf16 v[40:43], v[184:187], v[212:215], v[40:43]\n"
      "ds_read_b128 v[196:199], v248 offset:38912\n"
      "v_mfma_f32_16x16x32_bf16 v[44:47], v[184:187], v[216:219], v[44:47]\n"
      "ds_read_b128 v[200:203], v248 offset:40960\n"
      "v_mfma_f32_16x16x32_bf16 v[48:51], v[184:187], v[220:223], v[48:51]\n"
      "ds_read_b128 v[204:207], v248 offset:43008\n"
      "v_mfma_f32_16x16x32_bf16 v[52:55], v[184:187], v[224:227], v[52:55]\n"
      "ds_read_b128 v[208:211], v248 offset:45056\n"
      "v_mfma_f32_16x16x32_bf16 v[56:59], v[184:187], v[228:231], v[56:59]\n"
      "v_mfma_f32_16x16x32_bf16 v[60:63], v[188:191], v[212:215], v[60:63]\n"
      "v_mfma_f32_16x16x32_bf16 v[64:67], v[188:191], v[216:219], v[64:67]\n"
      "v_mfma_f32_16x16x32_bf16 v[68:71], v[188:191], v[220:223], v[68:71]\n"
      "v_mfma_f32_16x16x32_bf16 v[72:75], v[188:191], v[224:227], v[72:75]\n"
      "v_mfma_f32_16x16x32_bf16 v[76:79], v[188:191], v[228:231], v[76:79]\n"
      "s_waitcnt lgkmcnt(0)\n"
      "s_barrier\n"
      "s_waitcnt lgkmcnt(0)\n"
      "v_mfma_f32_16x16x32_bf16 v[0:3], v[160:163], v[192:195], v[0:3]\n"
      "ds_read_b64_tr_b16 v[176:177], v232 offset:16384\n"
      "v_mfma_f32_16x16x32_bf16 v[4:7], v[160:163], v[196:199], v[4:7]\n"
      "ds_read_b64_tr_b16 v[178:179], v232 offset:16896\n"
      "v_mfma_f32_16x16x32_bf16 v[8:11], v[160:163], v[200:203], v[8:11]\n"
      "ds_read_b64_tr_b16 v[180:181], v233 offset:16384\n"
      "v_mfma_f32_16x16x32_bf16 v[12:15], v[160:163], v[204:207], v[12:15]\n"
      "ds_read_b64_tr_b16 v[182:183], v233 offset:16896\n"
      "v_mfma_f32_16x16x32_bf16 v[16:19], v[160:163], v[208:211], v[16:19]\n"
      "ds_read_b64_tr_b16 v[184:185], v234 offset:16384\n"
      "v_mfma_f32_16x16x32_bf16 v[20:23], v[164:167], v[192:195], v[20:23]\n"
      "ds_read_b64_tr_b16 v[186:187], v234 offset:16896\n"
      "v_mfma_f32_16x16x32_bf16 v[24:27], v[164:167], v[196:199], v[24:27]\n"
      "ds_read_b64_tr_b16 v[188:189], v235 offset:16384\n"
      "v_mfma_f32_16x16x32_bf16 v[28:31], v[164:167], v[200:203], v[28:31]\n"
      "v_mfma_f32_16x16x32_bf16 v[32:35], v[164:167], v[204:207], v[32:35]\n"
      "ds_read_b64_tr_b16 v[190:191], v235 offset:16896\n"
      "v_mfma_f32_16x16x32_bf16 v[36:39], v[164:167], v[208:211], v[36:39]\n"
      "ds_read_b128 v[212:215], v249 offset:36864\n"
      "v_mfma_f32_16x16x32_bf16 v[40:43], v[168:171], v[192:195], v[40:43]\n"
      "ds_read_b128 v[216:219], v249 offset:38912\n"
      "v_mfma_f32_16x16x32_bf16 v[44:47], v[168:171], v[196:199], v[44:47]\n"
      "ds_read_b128 v[220:223], v249 offset:40960\n"
      "v_mfma_f32_16x16x32_bf16 v[48:51], v[168:171], v[200:203], v[48:51]\n"
      "ds_read_b128 v[224:227], v249 offset:43008\n"
      "v_mfma_f32_16x16x32_bf16 v[52:55], v[168:171], v[204:207], v[52:55]\n"
      "ds_read_b128 v[228:231], v249 offset:45056\n"
      "v_mfma_f32_16x16x32_bf16 v[56:59], v[168:171], v[208:211], v[56:59]\n"
      "v_mfma_f32_16x16x32_bf16 v[60:63], v[172:175], v[192:195], v[60:63]\n"
      "v_mfma_f32_16x16x32_bf16 v[64:67], v[172:175], v[196:199], v[64:67]\n"
      "v_mfma_f32_16x16x32_bf16 v[68:71], v[172:175], v[200:203], v[68:71]\n"
      "v_mfma_f32_16x16x32_bf16 v[72:75], v[172:175], v[204:207], v[72:75]\n"
      "v_mfma_f32_16x16x32_bf16 v[76:79], v[172:175], v[208:211], v[76:79]\n"
      "s_waitcnt lgkmcnt(0)\n"
      "v_mfma_f32_16x16x32_bf16 v[0:3], v[176:179], v[212:215], v[0:3]\n"
      "ds_read_b64_tr_b16 v[160:161], v232 offset:32768\n"
      "v_mfma_f32_16x16x32_bf16 v[4:7], v[176:179], v[216:219], v[4:7]\n"
      "ds_read_b64_tr_b16 v[162:163], v232 offset:33280\n"
      "v_mfma_f32_16x16x32_bf16 v[8:11], v[176:179], v[220:223], v[8:11]\n"
      "ds_read_b64_tr_b16 v[164:165], v233 offset:32768\n"
      "v_mfma_f32_16x16x32_bf16 v[12:15], v[176:179], v[224:227], v[12:15]\n"
      "ds_read_b64_tr_b16 v[166:167], v233 offset:33280\n"
      "v_mfma_f32_16x16x32_bf16 v[16:19], v[176:179], v[228:231], v[16:19]\n"
      "ds_read_b64_tr_b16 v[168:169], v234 offset:32768\n"
      "v_mfma_f32_16x16x32_bf16 v[20:23], v[180:183], v[212:215], v[20:23]\n"
      "ds_read_b64_tr_b16 v[170:171], v234 offset:33280\n"
      "v_mfma_f32_16x16x32_bf16 v[24:27], v[180:183], v[216:219], v[24:27]\n"
      "ds_read_b64_tr_b16 v[172:173], v235 offset:32768\n"
      "v_mfma_f32_16x16x32_bf16 v[28:31], v[180:183], v[220:223], v[28:31]\n"
      "v_mfma_f32_16x16x32_bf16 v[32:35], v[180:183], v[224:227], v[32:35]\n"
      "ds_read_b64_tr_b16 v[174:175], v235 offset:33280\n"
      "v_mfma_f32_16x16x32_bf16 v[36:39], v[180:183], v[228:231], v[36:39]\n"
      "ds_read_b128 v[192:195], v248 offset:46080\n"
      "v_mfma_f32_16x16x32_bf16 v[40:43], v[184:187], v[212:215], v[40:43]\n"
      "ds_read_b128 v[196:199], v248 offset:48128\n"
      "v_mfma_f32_16x16x32_bf16 v[44:47], v[184:187], v[216:219], v[44:47]\n"
      "ds_read_b128 v[200:203], v248 offset:50176\n"
      "v_mfma_f32_16x16x32_bf16 v[48:51], v[184:187], v[220:223], v[48:51]\n"
      "ds_read_b128 v[204:207], v248 offset:52224\n"
      "v_mfma_f32_16x16x32_bf16 v[52:55], v[184:187], v[224:227], v[52:55]\n"
      "ds_read_b128 v[208:211], v248 offset:54272\n"
      "v_mfma_f32_16x16x32_bf16 v[56:59], v[184:187], v[228:231], v[56:59]\n"
      "v_mfma_f32_16x16x32_bf16 v[60:63], v[188:191], v[212:215], v[60:63]\n"
      "v_mfma_f32_16x16x32_bf16 v[64:67], v[188:191], v[216:219], v[64:67]\n"
      "v_mfma_f32_16x16x32_bf16 v[68:71], v[188:191], v[220:223], v[68:71]\n"
      "v_mfma_f32_16x16x32_bf16 v[72:75], v[188:191], v[224:227], v[72:75]\n"
      "v_mfma_f32_16x16x32_bf16 v[76:79], v[188:191], v[228:231], v[76:79]\n"
      "s_waitcnt lgkmcnt(0)\n"
      "s_barrier\n"
      "s_waitcnt lgkmcnt(0)\n"
      "v_mfma_f32_16x16x32_bf16 v[0:3], v[160:163], v[192:195], v[0:3]\n"
      "ds_read_b64_tr_b16 v[176:177], v232 offset:49152\n"
      "v_mfma_f32_16x16x32_bf16 v[4:7], v[160:163], v[196:199], v[4:7]\n"
      "ds_read_b64_tr_b16 v[178:179], v232 offset:49664\n"
      "v_mfma_f32_16x16x32_bf16 v[8:11], v[160:163], v[200:203], v[8:11]\n"
      "ds_read_b64_tr_b16 v[180:181], v233 offset:49152\n"
      "v_mfma_f32_16x16x32_bf16 v[12:15], v[160:163], v[204:207], v[12:15]\n"
      "ds_read_b64_tr_b16 v[182:183], v233 offset:49664\n"
      "v_mfma_f32_16x16x32_bf16 v[16:19], v[160:163], v[208:211], v[16:19]\n"
      "ds_read_b64_tr_b16 v[184:185], v234 offset:49152\n"
      "v_mfma_f32_16x16x32_bf16 v[20:23], v[164:167], v[192:195], v[20:23]\n"
      "ds_read_b64_tr_b16 v[186:187], v234 offset:49664\n"
      "v_mfma_f32_16x16x32_bf16 v[24:27], v[164:167], v[196:199], v[24:27]\n"
      "ds_read_b64_tr_b16 v[188:189], v235 offset:49152\n"
      "v_mfma_f32_16x16x32_bf16 v[28:31], v[164:167], v[200:203], v[28:31]\n"
      "v_mfma_f32_16x16x32_bf16 v[32:35], v[164:167], v[204:207], v[32:35]\n"
      "ds_read_b64_tr_b16 v[190:191], v235 offset:49664\n"
      "v_mfma_f32_16x16x32_bf16 v[36:39], v[164:167], v[208:211], v[36:39]\n"
      "ds_read_b128 v[212:215], v249 offset:46080\n"
      "v_mfma_f32_16x16x32_bf16 v[40:43], v[168:171], v[192:195], v[40:43]\n"
      "ds_read_b128 v[216:219], v249 offset:48128\n"
      "v_mfma_f32_16x16x32_bf16 v[44:47], v[168:171], v[196:199], v[44:47]\n"
      "ds_read_b128 v[220:223], v249 offset:50176\n"
      "v_mfma_f32_16x16x32_bf16 v[48:51], v[168:171], v[200:203], v[48:51]\n"
      "ds_read_b128 v[224:227], v249 offset:52224\n"
      "v_mfma_f32_16x16x32_bf16 v[52:55], v[168:171], v[204:207], v[52:55]\n"
      "ds_read_b128 v[228:231], v249 offset:54272\n"
      "v_mfma_f32_16x16x32_bf16 v[56:59], v[168:171], v[208:211], v[56:59]\n"
      "v_mfma_f32_16x16x32_bf16 v[60:63], v[172:175], v[192:195], v[60:63]\n"
      "v_mfma_f32_16x16x32_bf16 v[64:67], v[172:175], v[196:199], v[64:67]\n"
      "v_mfma_f32_16x16x32_bf16 v[68:71], v[172:175], v[200:203], v[68:71]\n"
      "v_mfma_f32_16x16x32_bf16 v[72:75], v[172:175], v[204:207], v[72:75]\n"
      "v_mfma_f32_16x16x32_bf16 v[76:79], v[172:175], v[208:211], v[76:79]\n"
      "s_waitcnt lgkmcnt(0)\n"
      "v_mfma_f32_16x16x32_bf16 v[0:3], v[176:179], v[212:215], v[0:3]\n"
      "ds_read_b64_tr_b16 v[160:161], v236 offset:0\n"
      "v_mfma_f32_16x16x32_bf16 v[4:7], v[176:179], v[216:219], v[4:7]\n"
      "ds_read_b64_tr_b16 v[162:163], v236 offset:512\n"
      "v_mfma_f32_16x16x32_bf16 v[8:11], v[176:179], v[220:223], v[8:11]\n"
      "ds_read_b64_tr_b16 v[164:165], v237 offset:0\n"
      "v_mfma_f32_16x16x32_bf16 v[12:15], v[176:179], v[224:227], v[12:15]\n"
      "ds_read_b64_tr_b16 v[166:167], v237 offset:512\n"
      "v_mfma_f32_16x16x32_bf16 v[16:19], v[176:179], v[228:231], v[16:19]\n"
      "ds_read_b64_tr_b16 v[168:169], v238 offset:0\n"
      "v_mfma_f32_16x16x32_bf16 v[20:23], v[180:183], v[212:215], v[20:23]\n"
      "ds_read_b64_tr_b16 v[170:171], v238 offset:512\n"
      "v_mfma_f32_16x16x32_bf16 v[24:27], v[180:183], v[216:219], v[24:27]\n"
      "ds_read_b64_tr_b16 v[172:173], v239 offset:0\n"
      "v_mfma_f32_16x16x32_bf16 v[28:31], v[180:183], v[220:223], v[28:31]\n"
      "v_mfma_f32_16x16x32_bf16 v[32:35], v[180:183], v[224:227], v[32:35]\n"
      "ds_read_b64_tr_b16 v[174:175], v239 offset:512\n"
      "v_mfma_f32_16x16x32_bf16 v[36:39], v[180:183], v[228:231], v[36:39]\n"
      "ds_read_b128 v[192:195], v248 offset:55296\n"
      "v_mfma_f32_16x16x32_bf16 v[40:43], v[184:187], v[212:215], v[40:43]\n"
      "ds_read_b128 v[196:199], v248 offset:57344\n"
      "v_mfma_f32_16x16x32_bf16 v[44:47], v[184:187], v[216:219], v[44:47]\n"
      "ds_read_b128 v[200:203], v248 offset:59392\n"
      "v_mfma_f32_16x16x32_bf16 v[48:51], v[184:187], v[220:223], v[48:51]\n"
      "ds_read_b128 v[204:207], v248 offset:61440\n"
      "v_mfma_f32_16x16x32_bf16 v[52:55], v[184:187], v[224:227], v[52:55]\n"
      "ds_read_b128 v[208:211], v248 offset:63488\n"
      "v_mfma_f32_16x16x32_bf16 v[56:59], v[184:187], v[228:231], v[56:59]\n"
      "v_mfma_f32_16x16x32_bf16 v[60:63], v[188:191], v[212:215], v[60:63]\n"
      "v_mfma_f32_16x16x32_bf16 v[64:67], v[188:191], v[216:219], v[64:67]\n"
      "v_mfma_f32_16x16x32_bf16 v[68:71], v[188:191], v[220:223], v[68:71]\n"
      "v_mfma_f32_16x16x32_bf16 v[72:75], v[188:191], v[224:227], v[72:75]\n"
      "v_mfma_f32_16x16x32_bf16 v[76:79], v[188:191], v[228:231], v[76:79]\n"
      "s_waitcnt lgkmcnt(0)\n"
      "s_barrier\n"
      "s_waitcnt lgkmcnt(0)\n"
      "v_mfma_f32_16x16x32_bf16 v[0:3], v[160:163], v[192:195], v[0:3]\n"
      "ds_read_b64_tr_b16 v[176:177], v236 offset:16384\n"
      "v_mfma_f32_16x16x32_bf16 v[4:7], v[160:163], v[196:199], v[4:7]\n"
      "ds_read_b64_tr_b16 v[178:179], v236 offset:16896\n"
      "v_mfma_f32_16x16x32_bf16 v[8:11], v[160:163], v[200:203], v[8:11]\n"
      "ds_read_b64_tr_b16 v[180:181], v237 offset:16384\n"
      "v_mfma_f32_16x16x32_bf16 v[12:15], v[160:163], v[204:207], v[12:15]\n"
      "ds_read_b64_tr_b16 v[182:183], v237 offset:16896\n"
      "v_mfma_f32_16x16x32_bf16 v[16:19], v[160:163], v[208:211], v[16:19]\n"
      "ds_read_b64_tr_b16 v[184:185], v238 offset:16384\n"
      "v_mfma_f32_16x16x32_bf16 v[20:23], v[164:167], v[192:195], v[20:23]\n"
      "ds_read_b64_tr_b16 v[186:187], v238 offset:16896\n"
      "v_mfma_f32_16x16x32_bf16 v[24:27], v[164:167], v[196:199], v[24:27]\n"
      "ds_read_b64_tr_b16 v[188:189], v239 offset:16384\n"
      "v_mfma_f32_16x16x32_bf16 v[28:31], v[164:167], v[200:203], v[28:31]\n"
      "v_mfma_f32_16x16x32_bf16 v[32:35], v[164:167], v[204:207], v[32:35]\n"
      "ds_read_b64_tr_b16 v[190:191], v239 offset:16896\n"
      "v_mfma_f32_16x16x32_bf16 v[36:39], v[164:167], v[208:211], v[36:39]\n"
      "ds_read_b128 v[212:215], v249 offset:55296\n"
      "v_mfma_f32_16x16x32_bf16 v[40:43], v[168:171], v[192:195], v[40:43]\n"
      "ds_read_b128 v[216:219], v249 offset:57344\n"
      "v_mfma_f32_16x16x32_bf16 v[44:47], v[168:171], v[196:199], v[44:47]\n"
      "ds_read_b128 v[220:223], v249 offset:59392\n"
      "v_mfma_f32_16x16x32_bf16 v[48:51], v[168:171], v[200:203], v[48:51]\n"
      "ds_read_b128 v[224:227], v249 offset:61440\n"
      "v_mfma_f32_16x16x32_bf16 v[52:55], v[168:171], v[204:207], v[52:55]\n"
      "ds_read_b128 v[228:231], v249 offset:63488\n"
      "v_mfma_f32_16x16x32_bf16 v[56:59], v[168:171], v[208:211], v[56:59]\n"
      "v_mfma_f32_16x16x32_bf16 v[60:63], v[172:175], v[192:195], v[60:63]\n"
      "v_mfma_f32_16x16x32_bf16 v[64:67], v[172:175], v[196:199], v[64:67]\n"
      "v_mfma_f32_16x16x32_bf16 v[68:71], v[172:175], v[200:203], v[68:71]\n"
      "v_mfma_f32_16x16x32_bf16 v[72:75], v[172:175], v[204:207], v[72:75]\n"
      "v_mfma_f32_16x16x32_bf16 v[76:79], v[172:175], v[208:211], v[76:79]\n"
      "s_waitcnt lgkmcnt(0)\n"
      "s_barrier\n"
      "s_waitcnt lgkmcnt(0)\n"
      "v_mfma_f32_16x16x32_bf16 v[0:3], v[176:179], v[212:215], v[0:3]\n"
      "v_mfma_f32_16x16x32_bf16 v[4:7], v[176:179], v[216:219], v[4:7]\n"
      "v_mfma_f32_16x16x32_bf16 v[8:11], v[176:179], v[220:223], v[8:11]\n"
      "v_mfma_f32_16x16x32_bf16 v[12:15], v[176:179], v[224:227], v[12:15]\n"
      "v_mfma_f32_16x16x32_bf16 v[16:19], v[176:179], v[228:231], v[16:19]\n"
      "v_mfma_f32_16x16x32_bf16 v[20:23], v[180:183], v[212:215], v[20:23]\n"
      "v_mfma_f32_16x16x32_bf16 v[24:27], v[180:183], v[216:219], v[24:27]\n"
      "v_mfma_f32_16x16x32_bf16 v[28:31], v[180:183], v[220:223], v[28:31]\n"
      "v_mfma_f32_16x16x32_bf16 v[32:35], v[180:183], v[224:227], v[32:35]\n"
      "v_cvt_pk_bf16_f32 v96, v0, v1\n"
      "v_cvt_pk_bf16_f32 v97, v2, v3\n"
      "ds_write_b64 v244, v[96:97] offset:0\n"
      "v_mfma_f32_16x16x32_bf16 v[36:39], v[180:183], v[228:231], v[36:39]\n"
      "v_cvt_pk_bf16_f32 v98, v4, v5\n"
      "v_cvt_pk_bf16_f32 v99, v6, v7\n"
      "ds_write_b64 v244, v[98:99] offset:2048\n"
      "v_mfma_f32_16x16x32_bf16 v[40:43], v[184:187], v[212:215], v[40:43]\n"
      "v_cvt_pk_bf16_f32 v100, v8, v9\n"
      "v_cvt_pk_bf16_f32 v101, v10, v11\n"
      "ds_write_b64 v244, v[100:101] offset:4096\n"
      "v_mfma_f32_16x16x32_bf16 v[44:47], v[184:187], v[216:219], v[44:47]\n"
      "v_cvt_pk_bf16_f32 v102, v12, v13\n"
      "v_cvt_pk_bf16_f32 v103, v14, v15\n"
      "ds_write_b64 v244, v[102:103] offset:6144\n"
      "v_mfma_f32_16x16x32_bf16 v[48:51], v[184:187], v[220:223], v[48:51]\n"
      "v_cvt_pk_bf16_f32 v104, v16, v17\n"
      "v_cvt_pk_bf16_f32 v105, v18, v19\n"
      "ds_write_b64 v244, v[104:105] offset:8192\n"
      "v_mfma_f32_16x16x32_bf16 v[52:55], v[184:187], v[224:227], v[52:55]\n"
      "v_cvt_pk_bf16_f32 v106, v20, v21\n"
      "v_cvt_pk_bf16_f32 v107, v22, v23\n"
      "ds_write_b64 v245, v[106:107] offset:0\n"
      "v_mfma_f32_16x16x32_bf16 v[56:59], v[184:187], v[228:231], v[56:59]\n"
      "v_cvt_pk_bf16_f32 v108, v24, v25\n"
      "v_cvt_pk_bf16_f32 v109, v26, v27\n"
      "ds_write_b64 v245, v[108:109] offset:2048\n"
      "v_mfma_f32_16x16x32_bf16 v[60:63], v[188:191], v[212:215], v[60:63]\n"
      "v_cvt_pk_bf16_f32 v110, v28, v29\n"
      "v_cvt_pk_bf16_f32 v111, v30, v31\n"
      "ds_write_b64 v245, v[110:111] offset:4096\n"
      "v_mfma_f32_16x16x32_bf16 v[64:67], v[188:191], v[216:219], v[64:67]\n"
      "v_cvt_pk_bf16_f32 v96, v32, v33\n"
      "v_cvt_pk_bf16_f32 v97, v34, v35\n"
      "ds_write_b64 v245, v[96:97] offset:6144\n"
      "v_mfma_f32_16x16x32_bf16 v[68:71], v[188:191], v[220:223], v[68:71]\n"
      "v_cvt_pk_bf16_f32 v98, v36, v37\n"
      "v_cvt_pk_bf16_f32 v99, v38, v39\n"
      "ds_write_b64 v245, v[98:99] offset:8192\n"
      "v_mfma_f32_16x16x32_bf16 v[72:75], v[188:191], v[224:227], v[72:75]\n"
      "v_cvt_pk_bf16_f32 v100, v40, v41\n"
      "v_cvt_pk_bf16_f32 v101, v42, v43\n"
      "ds_write_b64 v246, v[100:101] offset:0\n"
      "v_mfma_f32_16x16x32_bf16 v[76:79], v[188:191], v[228:231], v[76:79]\n"
      "v_cvt_pk_bf16_f32 v102, v44, v45\n"
      "v_cvt_pk_bf16_f32 v103, v46, v47\n"
      "ds_write_b64 v246, v[102:103] offset:2048\n"
      "s_nop 7\n"
      "v_cvt_pk_bf16_f32 v104, v48, v49\n"
      "v_cvt_pk_bf16_f32 v105, v50, v51\n"
      "ds_write_b64 v246, v[104:105] offset:4096\n"
      "v_cvt_pk_bf16_f32 v106, v52, v53\n"
      "v_cvt_pk_bf16_f32 v107, v54, v55\n"
      "ds_write_b64 v246, v[106:107] offset:6144\n"
      "v_cvt_pk_bf16_f32 v108, v56, v57\n"
      "v_cvt_pk_bf16_f32 v109, v58, v59\n"
      "ds_write_b64 v246, v[108:109] offset:8192\n"
      "v_cvt_pk_bf16_f32 v110, v60, v61\n"
      "v_cvt_pk_bf16_f32 v111, v62, v63\n"
      "ds_write_b64 v247, v[110:111] offset:0\n"
      "v_cvt_pk_bf16_f32 v96, v64, v65\n"
      "v_cvt_pk_bf16_f32 v97, v66, v67\n"
      "ds_write_b64 v247, v[96:97] offset:2048\n"
      "v_cvt_pk_bf16_f32 v98, v68, v69\n"
      "v_cvt_pk_bf16_f32 v99, v70, v71\n"
      "ds_write_b64 v247, v[98:99] offset:4096\n"
      "v_cvt_pk_bf16_f32 v100, v72, v73\n"
      "v_cvt_pk_bf16_f32 v101, v74, v75\n"
      "ds_write_b64 v247, v[100:101] offset:6144\n"
      "v_cvt_pk_bf16_f32 v102, v76, v77\n"
      "v_cvt_pk_bf16_f32 v103, v78, v79\n"
      "ds_write_b64 v247, v[102:103] offset:8192\n"
      "v_xor_b32 v248, v252, v254\n"
      "v_lshlrev_b32 v248, 4, v248\n"
      "v_lshl_add_u32 v248, v253, 7, v248\n"
      "v_add_u32 v248, 120320, v248\n"
      "v_xor_b32 v249, 64, v248\n"
      "v_xor_b32 v232, v252, v254\n"
      "v_lshlrev_b32 v232, 4, v232\n"
      "v_lshl_add_u32 v232, v254, 7, v232\n"
      "v_lshrrev_b32 v255, 3, v253\n"
      "v_lshl_add_u32 v232, v255, 12, v232\n"
      "v_add_u32 v232, s55, v232\n"
      "v_xor_b32 v233, 64, v232\n"
      "v_lshrrev_b32 v234, 1, v252\n"
      "s_lshl_b32 s57, s52, 1\n"
      "v_or_b32 v234, s57, v234\n"
      "v_xor_b32 v234, v234, v254\n"
      "v_lshlrev_b32 v234, 4, v234\n"
      "v_lshl_add_u32 v234, v253, 7, v234\n"
      "v_and_b32 v255, 1, v252\n"
      "v_lshl_add_u32 v234, v255, 3, v234\n"
      "v_add_u32 v234, 131072, v234\n"
      "v_lshlrev_b32 v236, 4, v253\n"
      "v_add_u32 v236, 161280, v236\n"
      "s_lshl_b32 s57, s52, 2\n"
      "v_add_u32 v237, s57, v236\n"
      "s_add_u32 s95, s95, 65536\n"
      "s_waitcnt vmcnt(0)\n"
      "s_waitcnt lgkmcnt(0)\n"
      "s_barrier\n"
      "ds_read_b128 v[160:163], v232 offset:0\n"
      "ds_read_b128 v[0:3], v248 offset:0\n"
      "ds_read_b128 v[164:167], v233 offset:0\n"
      "ds_read_b128 v[4:7], v249 offset:0\n"
      "ds_read_b128 v[168:171], v232 offset:1024\n"
      "ds_read_b128 v[8:11], v248 offset:10240\n"
      "ds_read_b128 v[172:175], v233 offset:1024\n"
      "ds_read_b128 v[12:15], v249 offset:10240\n"
      "ds_read_b128 v[176:179], v232 offset:2048\n"
      "ds_read_b128 v[16:19], v248 offset:20480\n"
      "ds_read_b128 v[180:183], v233 offset:2048\n"
      "ds_read_b128 v[20:23], v249 offset:20480\n"
      "ds_read_b128 v[184:187], v232 offset:3072\n"
      "ds_read_b128 v[24:27], v248 offset:30720\n"
      "ds_read_b128 v[188:191], v233 offset:3072\n"
      "ds_read_b128 v[28:31], v249 offset:30720\n"
      "s_waitcnt lgkmcnt(14)\n"
      "v_mfma_f32_16x16x32_bf16 v[200:203], v[160:163], v[0:3], 0\n"
      "ds_read_b128 v[32:35], v248 offset:2048\n"
      "s_waitcnt lgkmcnt(13)\n"
      "v_mfma_f32_16x16x32_bf16 v[200:203], v[164:167], v[4:7], v[200:203]\n"
      "ds_read_b128 v[36:39], v249 offset:2048\n"
      "s_waitcnt lgkmcnt(12)\n"
      "v_mfma_f32_16x16x32_bf16 v[200:203], v[168:171], v[8:11], v[200:203]\n"
      "ds_read_b128 v[40:43], v248 offset:12288\n"
      "s_waitcnt lgkmcnt(11)\n"
      "v_mfma_f32_16x16x32_bf16 v[200:203], v[172:175], v[12:15], v[200:203]\n"
      "ds_read_b128 v[44:47], v249 offset:12288\n"
      "s_waitcnt lgkmcnt(10)\n"
      "v_mfma_f32_16x16x32_bf16 v[200:203], v[176:179], v[16:19], v[200:203]\n"
      "ds_read_b128 v[48:51], v248 offset:22528\n"
      "s_waitcnt lgkmcnt(9)\n"
      "v_mfma_f32_16x16x32_bf16 v[200:203], v[180:183], v[20:23], v[200:203]\n"
      "ds_read_b128 v[52:55], v249 offset:22528\n"
      "s_waitcnt lgkmcnt(8)\n"
      "v_mfma_f32_16x16x32_bf16 v[200:203], v[184:187], v[24:27], v[200:203]\n"
      "ds_read_b128 v[56:59], v248 offset:32768\n"
      "s_waitcnt lgkmcnt(7)\n"
      "v_mfma_f32_16x16x32_bf16 v[200:203], v[188:191], v[28:31], v[200:203]\n"
      "ds_read_b128 v[60:63], v249 offset:32768\n"
      "s_waitcnt lgkmcnt(7)\n"
      "v_mfma_f32_16x16x32_bf16 v[204:207], v[160:163], v[32:35], 0\n"
      "ds_read_b128 v[64:67], v248 offset:4096\n"
      "s_waitcnt lgkmcnt(7)\n"
      "v_mfma_f32_16x16x32_bf16 v[204:207], v[164:167], v[36:39], v[204:207]\n"
      "ds_read_b128 v[68:71], v249 offset:4096\n"
      "s_waitcnt lgkmcnt(7)\n"
      "v_mfma_f32_16x16x32_bf16 v[204:207], v[168:171], v[40:43], v[204:207]\n"
      "ds_read_b128 v[72:75], v248 offset:14336\n"
      "s_waitcnt lgkmcnt(7)\n"
      "v_mfma_f32_16x16x32_bf16 v[204:207], v[172:175], v[44:47], v[204:207]\n"
      "ds_read_b128 v[76:79], v249 offset:14336\n"
      "s_add_u32 m0, s55, 0\n"
      "s_add_u32 s57, s94, 0\n"
      "buffer_load_dwordx4 v250, s[36:39], s57 offen nt lds\n"
      "s_waitcnt lgkmcnt(7)\n"
      "v_mfma_f32_16x16x32_bf16 v[204:207], v[176:179], v[48:51], v[204:207]\n"
      "ds_read_b128 v[80:83], v248 offset:24576\n"
      "s_waitcnt lgkmcnt(7)\n"
      "v_mfma_f32_16x16x32_bf16 v[204:207], v[180:183], v[52:55], v[204:207]\n"
      "ds_read_b128 v[84:87], v249 offset:24576\n"
      "s_waitcnt lgkmcnt(7)\n"
      "v_mfma_f32_16x16x32_bf16 v[204:207], v[184:187], v[56:59], v[204:207]\n"
      "ds_read_b128 v[88:91], v248 offset:34816\n"
      "s_waitcnt lgkmcnt(7)\n"
      "v_mfma_f32_16x16x32_bf16 v[204:207], v[188:191], v[60:63], v[204:207]\n"
      "ds_read_b128 v[92:95], v249 offset:34816\n"
      "s_add_u32 m0, s55, 1024\n"
      "s_add_u32 s57, s94, 128\n"
      "buffer_load_dwordx4 v250, s[36:39], s57 offen nt lds\n"
      "s_waitcnt lgkmcnt(7)\n"
      "v_mfma_f32_16x16x32_bf16 v[208:211], v[160:163], v[64:67], 0\n"
      "ds_read_b128 v[96:99], v248 offset:6144\n"
      "s_waitcnt lgkmcnt(7)\n"
      "v_mfma_f32_16x16x32_bf16 v[208:211], v[164:167], v[68:71], v[208:211]\n"
      "ds_read_b128 v[100:103], v249 offset:6144\n"
      "s_waitcnt lgkmcnt(7)\n"
      "v_mfma_f32_16x16x32_bf16 v[208:211], v[168:171], v[72:75], v[208:211]\n"
      "ds_read_b128 v[104:107], v248 offset:16384\n"
      "s_waitcnt lgkmcnt(7)\n"
      "v_mfma_f32_16x16x32_bf16 v[208:211], v[172:175], v[76:79], v[208:211]\n"
      "ds_read_b128 v[108:111], v249 offset:16384\n"
      "s_add_u32 m0, s55, 2048\n"
      "s_add_u32 s57, s94, 256\n"
      "buffer_load_dwordx4 v250, s[36:39], s57 offen nt lds\n"
      "s_waitcnt lgkmcnt(7)\n"
      "v_mfma_f32_16x16x32_bf16 v[208:211], v[176:179], v[80:83], v[208:211]\n"
      "ds_read_b128 v[112:115], v248 offset:26624\n"
      "s_waitcnt lgkmcnt(7)\n"
      "v_mfma_f32_16x16x32_bf16 v[208:211], v[180:183], v[84:87], v[208:211]\n"
      "ds_read_b128 v[116:119], v249 offset:26624\n"
      "s_waitcnt lgkmcnt(7)\n"
      "v_mfma_f32_16x16x32_bf16 v[208:211], v[184:187], v[88:91], v[208:211]\n"
      "ds_read_b128 v[120:123], v248 offset:36864\n"
      "s_waitcnt lgkmcnt(7)\n"
      "v_mfma_f32_16x16x32_bf16 v[208:211], v[188:191], v[92:95], v[208:211]\n"
      "ds_read_b128 v[124:127], v249 offset:36864\n"
      "s_add_u32 m0, s55, 3072\n"
      "s_add_u32 s57, s94, 384\n"
      "buffer_load_dwordx4 v250, s[36:39], s57 offen nt lds\n"
      "s_waitcnt lgkmcnt(7)\n"
      "v_mfma_f32_16x16x32_bf16 v[212:215], v[160:163], v[96:99], 0\n"
      "ds_read_b128 v[128:131], v248 offset:8192\n"
      "s_waitcnt lgkmcnt(7)\n"
      "v_mfma_f32_16x16x32_bf16 v[212:215], v[164:167], v[100:103], v[212:215]\n"
      "ds_read_b128 v[132:135], v249 offset:8192\n"
      "s_waitcnt lgkmcnt(7)\n"
      "v_mfma_f32_16x16x32_bf16 v[212:215], v[168:171], v[104:107], v[212:215]\n"
      "ds_read_b128 v[136:139], v248 offset:18432\n"
      "s_waitcnt lgkmcnt(7)\n"
      "v_mfma_f32_16x16x32_bf16 v[212:215], v[172:175], v[108:111], v[212:215]\n"
      "ds_read_b128 v[140:143], v249 offset:18432\n"
      "s_add_u32 m0, s55, 4096\n"
      "s_add_u32 s57, s94, 4096\n"
      "buffer_load_dwordx4 v250, s[36:39], s57 offen nt lds\n"
      "s_waitcnt lgkmcnt(7)\n"
      "v_mfma_f32_16x16x32_bf16 v[212:215], v[176:179], v[112:115], v[212:215]\n"
      "ds_read_b128 v[144:147], v248 offset:28672\n"
      "s_waitcnt lgkmcnt(7)\n"
      "v_mfma_f32_16x16x32_bf16 v[212:215], v[180:183], v[116:119], v[212:215]\n"
      "ds_read_b128 v[148:151], v249 offset:28672\n"
      "s_waitcnt lgkmcnt(7)\n"
      "v_mfma_f32_16x16x32_bf16 v[212:215], v[184:187], v[120:123], v[212:215]\n"
      "ds_read_b128 v[152:155], v248 offset:38912\n"
      "s_waitcnt lgkmcnt(7)\n"
      "v_mfma_f32_16x16x32_bf16 v[212:215], v[188:191], v[124:127], v[212:215]\n"
      "ds_read_b128 v[156:159], v249 offset:38912\n"
      "s_add_u32 m0, s55, 5120\n"
      "s_add_u32 s57, s94, 4224\n"
      "buffer_load_dwordx4 v250, s[36:39], s57 offen nt lds\n"
      "s_waitcnt lgkmcnt(7)\n"
      "v_mfma_f32_16x16x32_bf16 v[216:219], v[160:163], v[128:131], 0\n"
      "s_waitcnt lgkmcnt(6)\n"
      "v_mfma_f32_16x16x32_bf16 v[216:219], v[164:167], v[132:135], v[216:219]\n"
      "s_waitcnt lgkmcnt(5)\n"
      "v_mfma_f32_16x16x32_bf16 v[216:219], v[168:171], v[136:139], v[216:219]\n"
      "s_waitcnt lgkmcnt(4)\n"
      "v_mfma_f32_16x16x32_bf16 v[216:219], v[172:175], v[140:143], v[216:219]\n"
      "s_add_u32 m0, s55, 6144\n"
      "s_add_u32 s57, s94, 4352\n"
      "buffer_load_dwordx4 v250, s[36:39], s57 offen nt lds\n"
      "s_waitcnt lgkmcnt(3)\n"
      "v_mfma_f32_16x16x32_bf16 v[216:219], v[176:179], v[144:147], v[216:219]\n"
      "s_waitcnt lgkmcnt(2)\n"
      "v_mfma_f32_16x16x32_bf16 v[216:219], v[180:183], v[148:151], v[216:219]\n"
      "s_waitcnt lgkmcnt(1)\n"
      "v_mfma_f32_16x16x32_bf16 v[216:219], v[184:187], v[152:155], v[216:219]\n"
      "s_waitcnt lgkmcnt(0)\n"
      "v_mfma_f32_16x16x32_bf16 v[216:219], v[188:191], v[156:159], v[216:219]\n"
      "s_add_u32 m0, s55, 7168\n"
      "s_add_u32 s57, s94, 4480\n"
      "buffer_load_dwordx4 v250, s[36:39], s57 offen nt lds\n"
      "s_add_u32 s94, s94, 65536\n"
      "s_nop 7\n"
      "v_max3_f32 v220, v200, v201, v202\n"
      "v_max_f32 v220, v220, v203\n"
      "v_max3_f32 v221, v204, v205, v206\n"
      "v_max_f32 v221, v221, v207\n"
      "v_max3_f32 v222, v208, v209, v210\n"
      "v_max_f32 v222, v222, v211\n"
      "v_max3_f32 v223, v212, v213, v214\n"
      "v_max_f32 v223, v223, v215\n"
      "v_max3_f32 v224, v216, v217, v218\n"
      "v_max_f32 v224, v224, v219\n"
      "v_mov_b32 v192, v220\n"
      "v_mov_b32 v193, v221\n"
      "v_mov_b32 v194, v222\n"
      "v_mov_b32 v195, v223\n"
      "v_mov_b32 v196, v224\n"
      "s_nop 0\n"
      "v_permlane16_swap_b32 v220, v192\n"
      "v_permlane16_swap_b32 v221, v193\n"
      "v_permlane16_swap_b32 v222, v194\n"
      "v_permlane16_swap_b32 v223, v195\n"
      "v_permlane16_swap_b32 v224, v196\n"
      "s_nop 0\n"
      "v_max_f32 v220, v220, v192\n"
      "v_max_f32 v221, v221, v193\n"
      "v_max_f32 v222, v222, v194\n"
      "v_max_f32 v223, v223, v195\n"
      "v_max_f32 v224, v224, v196\n"
      "v_mov_b32 v192, v220\n"
      "v_mov_b32 v193, v221\n"
      "v_mov_b32 v194, v222\n"
      "v_mov_b32 v195, v223\n"
      "v_mov_b32 v196, v224\n"
      "s_nop 0\n"
      "v_permlane32_swap_b32 v220, v192\n"
      "v_permlane32_swap_b32 v221, v193\n"
      "v_permlane32_swap_b32 v222, v194\n"
      "v_permlane32_swap_b32 v223, v195\n"
      "v_permlane32_swap_b32 v224, v196\n"
      "s_nop 0\n"
      "v_max_f32 v220, v220, v192\n"
      "v_max_f32 v221, v221, v193\n"
      "v_max_f32 v222, v222, v194\n"
      "v_max_f32 v223, v223, v195\n"
      "v_max_f32 v224, v224, v196\n"
      "ds_write_b32 v237, v220 offset:0\n"
      "ds_write_b32 v237, v221 offset:256\n"
      "ds_write_b32 v237, v222 offset:512\n"
      "ds_write_b32 v237, v223 offset:768\n"
      "ds_write_b32 v237, v224 offset:1024\n"
      "s_waitcnt lgkmcnt(0)\n"
      "s_barrier\n"
      "ds_read_b128 v[240:243], v236 offset:0\n"
      "ds_read_b128 v[244:247], v236 offset:256\n"
      "ds_read_b128 v[192:195], v236 offset:512\n"
      "ds_read_b128 v[196:199], v236 offset:768\n"
      "ds_read_b128 v[228:231], v236 offset:1024\n"
      "ds_read_b128 v[160:163], v232 offset:32768\n"
      "ds_read_b128 v[164:167], v233 offset:32768\n"
      "ds_read_b128 v[168:171], v232 offset:33792\n"
      "ds_read_b128 v[172:175], v233 offset:33792\n"
      "ds_read_b128 v[176:179], v232 offset:34816\n"
      "ds_read_b128 v[180:183], v233 offset:34816\n"
      "ds_read_b128 v[184:187], v232 offset:35840\n"
      "ds_read_b128 v[188:191], v233 offset:35840\n"
      "s_waitcnt lgkmcnt(12)\n"
      "v_max3_f32 v240, v240, v241, v242\n"
      "v_max_f32 v240, v240, v243\n"
      "v_sub_f32 v200, v200, v240\n"
      "v_sub_f32 v201, v201, v240\n"
      "v_sub_f32 v202, v202, v240\n"
      "v_sub_f32 v203, v203, v240\n"
      "v_mul_f32 v200, s98, v200\n"
      "v_mul_f32 v201, s98, v201\n"
      "v_mul_f32 v202, s98, v202\n"
      "v_mul_f32 v203, s98, v203\n"
      "v_exp_f32 v200, v200\n"
      "v_exp_f32 v201, v201\n"
      "v_exp_f32 v202, v202\n"
      "v_exp_f32 v203, v203\n"
      "s_nop 0\n"
      "v_add_f32 v220, v200, v201\n"
      "v_add_f32 v241, v202, v203\n"
      "v_cvt_pk_bf16_f32 v242, v200, v201\n"
      "v_cvt_pk_bf16_f32 v243, v202, v203\n"
      "v_add_f32 v220, v220, v241\n"
      "ds_write_b64 v234, v[242:243] offset:0\n"
      "v_sub_f32 v200, 0, v240\n"
      "v_sub_f32 v201, 0, v240\n"
      "v_sub_f32 v202, 0, v240\n"
      "v_sub_f32 v203, 0, v240\n"
      "s_waitcnt lgkmcnt(12)\n"
      "v_max3_f32 v244, v244, v245, v246\n"
      "v_max_f32 v244, v244, v247\n"
      "v_sub_f32 v204, v204, v244\n"
      "v_sub_f32 v205, v205, v244\n"
      "v_sub_f32 v206, v206, v244\n"
      "v_sub_f32 v207, v207, v244\n"
      "v_mul_f32 v204, s98, v204\n"
      "v_mul_f32 v205, s98, v205\n"
      "v_mul_f32 v206, s98, v206\n"
      "v_mul_f32 v207, s98, v207\n"
      "v_exp_f32 v204, v204\n"
      "v_exp_f32 v205, v205\n"
      "v_exp_f32 v206, v206\n"
      "v_exp_f32 v207, v207\n"
      "s_nop 0\n"
      "v_add_f32 v221, v204, v205\n"
      "v_add_f32 v245, v206, v207\n"
      "v_cvt_pk_bf16_f32 v246, v204, v205\n"
      "v_cvt_pk_bf16_f32 v247, v206, v207\n"
      "v_add_f32 v221, v221, v245\n"
      "ds_write_b64 v234, v[246:247] offset:2048\n"
      "v_sub_f32 v204, 0, v244\n"
      "v_sub_f32 v205, 0, v244\n"
      "v_sub_f32 v206, 0, v244\n"
      "v_sub_f32 v207, 0, v244\n"
      "s_waitcnt lgkmcnt(12)\n"
      "v_max3_f32 v192, v192, v193, v194\n"
      "v_max_f32 v192, v192, v195\n"
      "v_sub_f32 v208, v208, v192\n"
      "v_sub_f32 v209, v209, v192\n"
      "v_sub_f32 v210, v210, v192\n"
      "v_sub_f32 v211, v211, v192\n"
      "v_mul_f32 v208, s98, v208\n"
      "v_mul_f32 v209, s98, v209\n"
      "v_mul_f32 v210, s98, v210\n"
      "v_mul_f32 v211, s98, v211\n"
      "v_exp_f32 v208, v208\n"
      "v_exp_f32 v209, v209\n"
      "v_exp_f32 v210, v210\n"
      "v_exp_f32 v211, v211\n"
      "s_nop 0\n"
      "v_add_f32 v222, v208, v209\n"
      "v_add_f32 v193, v210, v211\n"
      "v_cvt_pk_bf16_f32 v194, v208, v209\n"
      "v_cvt_pk_bf16_f32 v195, v210, v211\n"
      "v_add_f32 v222, v222, v193\n"
      "ds_write_b64 v234, v[194:195] offset:4096\n"
      "v_sub_f32 v208, 0, v192\n"
      "v_sub_f32 v209, 0, v192\n"
      "v_sub_f32 v210, 0, v192\n"
      "v_sub_f32 v211, 0, v192\n"
      "s_waitcnt lgkmcnt(12)\n"
      "v_max3_f32 v196, v196, v197, v198\n"
      "v_max_f32 v196, v196, v199\n"
      "v_sub_f32 v212, v212, v196\n"
      "v_sub_f32 v213, v213, v196\n"
      "v_sub_f32 v214, v214, v196\n"
      "v_sub_f32 v215, v215, v196\n"
      "v_mul_f32 v212, s98, v212\n"
      "v_mul_f32 v213, s98, v213\n"
      "v_mul_f32 v214, s98, v214\n"
      "v_mul_f32 v215, s98, v215\n"
      "v_exp_f32 v212, v212\n"
      "v_exp_f32 v213, v213\n"
      "v_exp_f32 v214, v214\n"
      "v_exp_f32 v215, v215\n"
      "s_nop 0\n"
      "v_add_f32 v223, v212, v213\n"
      "v_add_f32 v197, v214, v215\n"
      "v_cvt_pk_bf16_f32 v198, v212, v213\n"
      "v_cvt_pk_bf16_f32 v199, v214, v215\n"
      "v_add_f32 v223, v223, v197\n"
      "ds_write_b64 v234, v[198:199] offset:6144\n"
      "v_sub_f32 v212, 0, v196\n"
      "v_sub_f32 v213, 0, v196\n"
      "v_sub_f32 v214, 0, v196\n"
      "v_sub_f32 v215, 0, v196\n"
      "s_waitcnt lgkmcnt(12)\n"
      "v_max3_f32 v228, v228, v229, v230\n"
      "v_max_f32 v228, v228, v231\n"
      "v_sub_f32 v216, v216, v228\n"
      "v_sub_f32 v217, v217, v228\n"
      "v_sub_f32 v218, v218, v228\n"
      "v_sub_f32 v219, v219, v228\n"
      "v_mul_f32 v216, s98, v216\n"
      "v_mul_f32 v217, s98, v217\n"
      "v_mul_f32 v218, s98, v218\n"
      "v_mul_f32 v219, s98, v219\n"
      "v_exp_f32 v216, v216\n"
      "v_exp_f32 v217, v217\n"
      "v_exp_f32 v218, v218\n"
      "v_exp_f32 v219, v219\n"
      "s_nop 0\n"
      "v_add_f32 v224, v216, v217\n"
      "v_add_f32 v229, v218, v219\n"
      "v_cvt_pk_bf16_f32 v230, v216, v217\n"
      "v_cvt_pk_bf16_f32 v231, v218, v219\n"
      "v_add_f32 v224, v224, v229\n"
      "ds_write_b64 v234, v[230:231] offset:8192\n"
      "v_sub_f32 v216, 0, v228\n"
      "v_sub_f32 v217, 0, v228\n"
      "v_sub_f32 v218, 0, v228\n"
      "v_sub_f32 v219, 0, v228\n"
      "s_waitcnt lgkmcnt(0)\n"
      "s_barrier\n"
      "v_mfma_f32_16x16x32_bf16 v[192:195], v[160:163], v[0:3], v[200:203]\n"
      "v_mfma_f32_16x16x32_bf16 v[192:195], v[164:167], v[4:7], v[192:195]\n"
      "v_mfma_f32_16x16x32_bf16 v[192:195], v[168:171], v[8:11], v[192:195]\n"
      "v_mfma_f32_16x16x32_bf16 v[192:195], v[172:175], v[12:15], v[192:195]\n"
      "v_mfma_f32_16x16x32_bf16 v[192:195], v[176:179], v[16:19], v[192:195]\n"
      "v_mfma_f32_16x16x32_bf16 v[192:195], v[180:183], v[20:23], v[192:195]\n"
      "v_mfma_f32_16x16x32_bf16 v[192:195], v[184:187], v[24:27], v[192:195]\n"
      "v_mfma_f32_16x16x32_bf16 v[192:195], v[188:191], v[28:31], v[192:195]\n"
      "s_mov_b32 s54, 0\n"
      "s_nop 1\n"
      "LQK%=:\n"
      "v_mfma_f32_16x16x32_bf16 v[196:199], v[160:163], v[32:35], v[204:207]\n"
      "s_add_u32 m0, s55, 32768\n"
      "s_add_u32 s57, s95, 0\n"
      "buffer_load_dwordx4 v250, s[36:39], s57 offen nt lds\n"
      "v_mfma_f32_16x16x32_bf16 v[196:199], v[164:167], v[36:39], v[196:199]\n"
      "v_mfma_f32_16x16x32_bf16 v[196:199], v[168:171], v[40:43], v[196:199]\n"
      "v_mul_f32 v192, s98, v192\n"
      "v_mul_f32 v193, s98, v193\n"
      "v_exp_f32 v192, v192\n"
      "v_mfma_f32_16x16x32_bf16 v[196:199], v[172:175], v[44:47], v[196:199]\n"
      "s_add_u32 m0, s55, 33792\n"
      "s_add_u32 s57, s95, 128\n"
      "buffer_load_dwordx4 v250, s[36:39], s57 offen nt lds\n"
      "v_mul_f32 v194, s98, v194\n"
      "v_mul_f32 v195, s98, v195\n"
      "v_exp_f32 v193, v193\n"
      "v_mfma_f32_16x16x32_bf16 v[196:199], v[176:179], v[48:51], v[196:199]\n"
      "v_exp_f32 v194, v194\n"
      "v_add_f32 v220, v220, v192\n"
      "v_mfma_f32_16x16x32_bf16 v[196:199], v[180:183], v[52:55], v[196:199]\n"
      "v_exp_f32 v195, v195\n"
      "v_add_f32 v220, v220, v193\n"
      "v_mfma_f32_16x16x32_bf16 v[196:199], v[184:187], v[56:59], v[196:199]\n"
      "s_add_u32 m0, s55, 34816\n"
      "s_add_u32 s57, s95, 256\n"
      "buffer_load_dwordx4 v250, s[36:39], s57 offen nt lds\n"
      "v_cvt_pk_bf16_f32 v228, v192, v193\n"
      "v_add_f32 v220, v220, v194\n"
      "v_mfma_f32_16x16x32_bf16 v[196:199], v[188:191], v[60:63], v[196:199]\n"
      "v_cvt_pk_bf16_f32 v229, v194, v195\n"
      "v_add_f32 v220, v220, v195\n"
      "ds_write_b64 v234, v[228:229] offset:10240\n"
      "v_mfma_f32_16x16x32_bf16 v[192:195], v[160:163], v[64:67], v[208:211]\n"
      "v_mfma_f32_16x16x32_bf16 v[192:195], v[164:167], v[68:71], v[192:195]\n"
      "s_add_u32 m0, s55, 35840\n"
      "s_add_u32 s57, s95, 384\n"
      "buffer_load_dwordx4 v250, s[36:39], s57 offen nt lds\n"
      "v_mfma_f32_16x16x32_bf16 v[192:195], v[168:171], v[72:75], v[192:195]\n"
      "v_mul_f32 v196, s98, v196\n"
      "v_mul_f32 v197, s98, v197\n"
      "v_exp_f32 v196, v196\n"
      "v_mfma_f32_16x16x32_bf16 v[192:195], v[172:175], v[76:79], v[192:195]\n"
      "v_mul_f32 v198, s98, v198\n"
      "v_mul_f32 v199, s98, v199\n"
      "v_exp_f32 v197, v197\n"
      "v_mfma_f32_16x16x32_bf16 v[192:195], v[176:179], v[80:83], v[192:195]\n"
      "s_add_u32 m0, s55, 36864\n"
      "s_add_u32 s57, s95, 4096\n"
      "buffer_load_dwordx4 v250, s[36:39], s57 offen nt lds\n"
      "v_exp_f32 v198, v198\n"
      "v_add_f32 v221, v221, v196\n"
      "v_mfma_f32_16x16x32_bf16 v[192:195], v[180:183], v[84:87], v[192:195]\n"
      "v_exp_f32 v199, v199\n"
      "v_add_f32 v221, v221, v197\n"
      "v_mfma_f32_16x16x32_bf16 v[192:195], v[184:187], v[88:91], v[192:195]\n"
      "v_cvt_pk_bf16_f32 v226, v196, v197\n"
      "v_add_f32 v221, v221, v198\n"
      "v_mfma_f32_16x16x32_bf16 v[192:195], v[188:191], v[92:95], v[192:195]\n"
      "s_add_u32 m0, s55, 37888\n"
      "s_add_u32 s57, s95, 4224\n"
      "buffer_load_dwordx4 v250, s[36:39], s57 offen nt lds\n"
      "v_cvt_pk_bf16_f32 v227, v198, v199\n"
      "v_add_f32 v221, v221, v199\n"
      "ds_write_b64 v234, v[226:227] offset:12288\n"
      "v_mfma_f32_16x16x32_bf16 v[196:199], v[160:163], v[96:99], v[212:215]\n"
      "v_mfma_f32_16x16x32_bf16 v[196:199], v[164:167], v[100:103], v[196:199]\n"
      "v_mfma_f32_16x16x32_bf16 v[196:199], v[168:171], v[104:107], v[196:199]\n"
      "s_add_u32 m0, s55, 38912\n"
      "s_add_u32 s57, s95, 4352\n"
      "buffer_load_dwordx4 v250, s[36:39], s57 offen nt lds\n"
      "v_mul_f32 v192, s98, v192\n"
      "v_mul_f32 v193, s98, v193\n"
      "v_exp_f32 v192, v192\n"
      "v_mfma_f32_16x16x32_bf16 v[196:199], v[172:175], v[108:111], v[196:199]\n"
      "v_mul_f32 v194, s98, v194\n"
      "v_mul_f32 v195, s98, v195\n"
      "v_exp_f32 v193, v193\n"
      "v_mfma_f32_16x16x32_bf16 v[196:199], v[176:179], v[112:115], v[196:199]\n"
      "v_exp_f32 v194, v194\n"
      "v_add_f32 v222, v222, v192\n"
      "v_mfma_f32_16x16x32_bf16 v[196:199], v[180:183], v[116:119], v[196:199]\n"
      "s_add_u32 m0, s55, 39936\n"
      "s_add_u32 s57, s95, 4480\n"
      "buffer_load_dwordx4 v250, s[36:39], s57 offen nt lds\n"
      "s_add_u32 s95, s95, 65536\n"
      "v_exp_f32 v195, v195\n"
      "v_add_f32 v222, v222, v193\n"
      "v_mfma_f32_16x16x32_bf16 v[196:199], v[184:187], v[120:123], v[196:199]\n"
      "v_cvt_pk_bf16_f32 v228, v192, v193\n"
      "v_add_f32 v222, v222, v194\n"
      "v_mfma_f32_16x16x32_bf16 v[196:199], v[188:191], v[124:127], v[196:199]\n"
      "v_cvt_pk_bf16_f32 v229, v194, v195\n"
      "v_add_f32 v222, v222, v195\n"
      "ds_write_b64 v234, v[228:229] offset:14336\n"
      "s_waitcnt vmcnt(8)\n"
      "v_mfma_f32_16x16x32_bf16 v[192:195], v[160:163], v[128:131], v[216:219]\n"
      "ds_read_b128 v[160:163], v232 offset:0\n"
      "v_mfma_f32_16x16x32_bf16 v[192:195], v[164:167], v[132:135], v[192:195]\n"
      "ds_read_b128 v[164:167], v233 offset:0\n"
      "v_mfma_f32_16x16x32_bf16 v[192:195], v[168:171], v[136:139], v[192:195]\n"
      "ds_read_b128 v[168:171], v232 offset:1024\n"
      "v_mul_f32 v196, s98, v196\n"
      "v_mul_f32 v197, s98, v197\n"
      "v_exp_f32 v196, v196\n"
      "v_mfma_f32_16x16x32_bf16 v[192:195], v[172:175], v[140:143], v[192:195]\n"
      "ds_read_b128 v[172:175], v233 offset:1024\n"
      "v_mul_f32 v198, s98, v198\n"
      "v_mul_f32 v199, s98, v199\n"
      "v_exp_f32 v197, v197\n"
      "v_mfma_f32_16x16x32_bf16 v[192:195], v[176:179], v[144:147], v[192:195]\n"
      "ds_read_b128 v[176:179], v232 offset:2048\n"
      "v_exp_f32 v198, v198\n"
      "v_add_f32 v223, v223, v196\n"
      "v_mfma_f32_16x16x32_bf16 v[192:195], v[180:183], v[148:151], v[192:195]\n"
      "ds_read_b128 v[180:183], v233 offset:2048\n"
      "v_exp_f32 v199, v199\n"
      "v_add_f32 v223, v223, v197\n"
      "v_mfma_f32_16x16x32_bf16 v[192:195], v[184:187], v[152:155], v[192:195]\n"
      "ds_read_b128 v[184:187], v232 offset:3072\n"
      "v_cvt_pk_bf16_f32 v226, v196, v197\n"
      "v_add_f32 v223, v223, v198\n"
      "v_mfma_f32_16x16x32_bf16 v[192:195], v[188:191], v[156:159], v[192:195]\n"
      "ds_read_b128 v[188:191], v233 offset:3072\n"
      "v_cvt_pk_bf16_f32 v227, v198, v199\n"
      "v_add_f32 v223, v223, v199\n"
      "ds_write_b64 v234, v[226:227] offset:16384\n"
      "s_waitcnt lgkmcnt(8)\n"
      "v_mfma_f32_16x16x32_bf16 v[196:199], v[160:163], v[0:3], v[200:203]\n"
      "s_waitcnt lgkmcnt(7)\n"
      "v_mfma_f32_16x16x32_bf16 v[196:199], v[164:167], v[4:7], v[196:199]\n"
      "s_waitcnt lgkmcnt(6)\n"
      "v_mfma_f32_16x16x32_bf16 v[196:199], v[168:171], v[8:11], v[196:199]\n"
      "v_mul_f32 v192, s98, v192\n"
      "v_mul_f32 v193, s98, v193\n"
      "v_exp_f32 v192, v192\n"
      "s_waitcnt lgkmcnt(5)\n"
      "v_mfma_f32_16x16x32_bf16 v[196:199], v[172:175], v[12:15], v[196:199]\n"
      "v_mul_f32 v194, s98, v194\n"
      "v_mul_f32 v195, s98, v195\n"
      "v_exp_f32 v193, v193\n"
      "s_waitcnt lgkmcnt(4)\n"
      "v_mfma_f32_16x16x32_bf16 v[196:199], v[176:179], v[16:19], v[196:199]\n"
      "v_exp_f32 v194, v194\n"
      "v_add_f32 v224, v224, v192\n"
      "s_waitcnt lgkmcnt(3)\n"
      "v_mfma_f32_16x16x32_bf16 v[196:199], v[180:183], v[20:23], v[196:199]\n"
      "v_exp_f32 v195, v195\n"
      "v_add_f32 v224, v224, v193\n"
      "s_waitcnt lgkmcnt(2)\n"
      "v_mfma_f32_16x16x32_bf16 v[196:199], v[184:187], v[24:27], v[196:199]\n"
      "v_cvt_pk_bf16_f32 v228, v192, v193\n"
      "v_add_f32 v224, v224, v194\n"
      "s_waitcnt lgkmcnt(1)\n"
      "v_mfma_f32_16x16x32_bf16 v[196:199], v[188:191], v[28:31], v[196:199]\n"
      "v_cvt_pk_bf16_f32 v229, v194, v195\n"
      "v_add_f32 v224, v224, v195\n"
      "ds_write_b64 v234, v[228:229] offset:18432\n"
      "s_waitcnt lgkmcnt(0)\n"
      "s_barrier\n"
      "v_mfma_f32_16x16x32_bf16 v[192:195], v[160:163], v[32:35], v[204:207]\n"
      "s_add_u32 m0, s55, 0\n"
      "s_add_u32 s57, s94, 0\n"
      "buffer_load_dwordx4 v250, s[36:39], s57 offen nt lds\n"
      "v_mfma_f32_16x16x32_bf16 v[192:195], v[164:167], v[36:39], v[192:195]\n"
      "v_mfma_f32_16x16x32_bf16 v[192:195], v[168:171], v[40:43], v[192:195]\n"
      "v_mul_f32 v196, s98, v196\n"
      "v_mul_f32 v197, s98, v197\n"
      "v_exp_f32 v196, v196\n"
      "v_mfma_f32_16x16x32_bf16 v[192:195], v[172:175], v[44:47], v[192:195]\n"
      "s_add_u32 m0, s55, 1024\n"
      "s_add_u32 s57, s94, 128\n"
      "buffer_load_dwordx4 v250, s[36:39], s57 offen nt lds\n"
      "v_mul_f32 v198, s98, v198\n"
      "v_mul_f32 v199, s98, v199\n"
      "v_exp_f32 v197, v197\n"
      "v_mfma_f32_16x16x32_bf16 v[192:195], v[176:179], v[48:51], v[192:195]\n"
      "v_exp_f32 v198, v198\n"
      "v_add_f32 v220, v220, v196\n"
      "v_mfma_f32_16x16x32_bf16 v[192:195], v[180:183], v[52:55], v[192:195]\n"
      "v_exp_f32 v199, v199\n"
      "v_add_f32 v220, v220, v197\n"
      "v_mfma_f32_16x16x32_bf16 v[192:195], v[184:187], v[56:59], v[192:195]\n"
      "s_add_u32 m0, s55, 2048\n"
      "s_add_u32 s57, s94, 256\n"
      "buffer_load_dwordx4 v250, s[36:39], s57 offen nt lds\n"
      "v_cvt_pk_bf16_f32 v228, v196, v197\n"
      "v_add_f32 v220, v220, v198\n"
      "v_mfma_f32_16x16x32_bf16 v[192:195], v[188:191], v[60:63], v[192:195]\n"
      "v_cvt_pk_bf16_f32 v229, v198, v199\n"
      "v_add_f32 v220, v220, v199\n"
      "ds_write_b64 v234, v[228:229] offset:0\n"
      "v_mfma_f32_16x16x32_bf16 v[196:199], v[160:163], v[64:67], v[208:211]\n"
      "v_mfma_f32_16x16x32_bf16 v[196:199], v[164:167], v[68:71], v[196:199]\n"
      "s_add_u32 m0, s55, 3072\n"
      "s_add_u32 s57, s94, 384\n"
      "buffer_load_dwordx4 v250, s[36:39], s57 offen nt lds\n"
      "v_mfma_f32_16x16x32_bf16 v[196:199], v[168:171], v[72:75], v[196:199]\n"
      "v_mul_f32 v192, s98, v192\n"
      "v_mul_f32 v193, s98, v193\n"
      "v_exp_f32 v192, v192\n"
      "v_mfma_f32_16x16x32_bf16 v[196:199], v[172:175], v[76:79], v[196:199]\n"
      "v_mul_f32 v194, s98, v194\n"
      "v_mul_f32 v195, s98, v195\n"
      "v_exp_f32 v193, v193\n"
      "v_mfma_f32_16x16x32_bf16 v[196:199], v[176:179], v[80:83], v[196:199]\n"
      "s_add_u32 m0, s55, 4096\n"
      "s_add_u32 s57, s94, 4096\n"
      "buffer_load_dwordx4 v250, s[36:39], s57 offen nt lds\n"
      "v_exp_f32 v194, v194\n"
      "v_add_f32 v221, v221, v192\n"
      "v_mfma_f32_16x16x32_bf16 v[196:199], v[180:183], v[84:87], v[196:199]\n"
      "v_exp_f32 v195, v195\n"
      "v_add_f32 v221, v221, v193\n"
      "v_mfma_f32_16x16x32_bf16 v[196:199], v[184:187], v[88:91], v[196:199]\n"
      "v_cvt_pk_bf16_f32 v226, v192, v193\n"
      "v_add_f32 v221, v221, v194\n"
      "v_mfma_f32_16x16x32_bf16 v[196:199], v[188:191], v[92:95], v[196:199]\n"
      "s_add_u32 m0, s55, 5120\n"
      "s_add_u32 s57, s94, 4224\n"
      "buffer_load_dwordx4 v250, s[36:39], s57 offen nt lds\n"
      "v_cvt_pk_bf16_f32 v227, v194, v195\n"
      "v_add_f32 v221, v221, v195\n"
      "ds_write_b64 v234, v[226:227] offset:2048\n"
      "v_mfma_f32_16x16x32_bf16 v[192:195], v[160:163], v[96:99], v[212:215]\n"
      "v_mfma_f32_16x16x32_bf16 v[192:195], v[164:167], v[100:103], v[192:195]\n"
      "v_mfma_f32_16x16x32_bf16 v[192:195], v[168:171], v[104:107], v[192:195]\n"
      "s_add_u32 m0, s55, 6144\n"
      "s_add_u32 s57, s94, 4352\n"
      "buffer_load_dwordx4 v250, s[36:39], s57 offen nt lds\n"
      "v_mul_f32 v196, s98, v196\n"
      "v_mul_f32 v197, s98, v197\n"
      "v_exp_f32 v196, v196\n"
      "v_mfma_f32_16x16x32_bf16 v[192:195], v[172:175], v[108:111], v[192:195]\n"
      "v_mul_f32 v198, s98, v198\n"
      "v_mul_f32 v199, s98, v199\n"
      "v_exp_f32 v197, v197\n"
      "v_mfma_f32_16x16x32_bf16 v[192:195], v[176:179], v[112:115], v[192:195]\n"
      "v_exp_f32 v198, v198\n"
      "v_add_f32 v222, v222, v196\n"
      "v_mfma_f32_16x16x32_bf16 v[192:195], v[180:183], v[116:119], v[192:195]\n"
      "s_add_u32 m0, s55, 7168\n"
      "s_add_u32 s57, s94, 4480\n"
      "buffer_load_dwordx4 v250, s[36:39], s57 offen nt lds\n"
      "s_add_u32 s94, s94, 65536\n"
      "v_exp_f32 v199, v199\n"
      "v_add_f32 v222, v222, v197\n"
      "v_mfma_f32_16x16x32_bf16 v[192:195], v[184:187], v[120:123], v[192:195]\n"
      "v_cvt_pk_bf16_f32 v228, v196, v197\n"
      "v_add_f32 v222, v222, v198\n"
      "v_mfma_f32_16x16x32_bf16 v[192:195], v[188:191], v[124:127], v[192:195]\n"
      "v_cvt_pk_bf16_f32 v229, v198, v199\n"
      "v_add_f32 v222, v222, v199\n"
      "ds_write_b64 v234, v[228:229] offset:4096\n"
      "s_waitcnt vmcnt(8)\n"
      "v_mfma_f32_16x16x32_bf16 v[196:199], v[160:163], v[128:131], v[216:219]\n"
      "ds_read_b128 v[160:163], v232 offset:32768\n"
      "v_mfma_f32_16x16x32_bf16 v[196:199], v[164:167], v[132:135], v[196:199]\n"
      "ds_read_b128 v[164:167], v233 offset:32768\n"
      "v_mfma_f32_16x16x32_bf16 v[196:199], v[168:171], v[136:139], v[196:199]\n"
      "ds_read_b128 v[168:171], v232 offset:33792\n"
      "v_mul_f32 v192, s98, v192\n"
      "v_mul_f32 v193, s98, v193\n"
      "v_exp_f32 v192, v192\n"
      "v_mfma_f32_16x16x32_bf16 v[196:199], v[172:175], v[140:143], v[196:199]\n"
      "ds_read_b128 v[172:175], v233 offset:33792\n"
      "v_mul_f32 v194, s98, v194\n"
      "v_mul_f32 v195, s98, v195\n"
      "v_exp_f32 v193, v193\n"
      "v_mfma_f32_16x16x32_bf16 v[196:199], v[176:179], v[144:147], v[196:199]\n"
      "ds_read_b128 v[176:179], v232 offset:34816\n"
      "v_exp_f32 v194, v194\n"
      "v_add_f32 v223, v223, v192\n"
      "v_mfma_f32_16x16x32_bf16 v[196:199], v[180:183], v[148:151], v[196:199]\n"
      "ds_read_b128 v[180:183], v233 offset:34816\n"
      "v_exp_f32 v195, v195\n"
      "v_add_f32 v223, v223, v193\n"
      "v_mfma_f32_16x16x32_bf16 v[196:199], v[184:187], v[152:155], v[196:199]\n"
      "ds_read_b128 v[184:187], v232 offset:35840\n"
      "v_cvt_pk_bf16_f32 v226, v192, v193\n"
      "v_add_f32 v223, v223, v194\n"
      "v_mfma_f32_16x16x32_bf16 v[196:199], v[188:191], v[156:159], v[196:199]\n"
      "ds_read_b128 v[188:191], v233 offset:35840\n"
      "v_cvt_pk_bf16_f32 v227, v194, v195\n"
      "v_add_f32 v223, v223, v195\n"
      "ds_write_b64 v234, v[226:227] offset:6144\n"
      "s_waitcnt lgkmcnt(8)\n"
      "v_mfma_f32_16x16x32_bf16 v[192:195], v[160:163], v[0:3], v[200:203]\n"
      "s_waitcnt lgkmcnt(7)\n"
      "v_mfma_f32_16x16x32_bf16 v[192:195], v[164:167], v[4:7], v[192:195]\n"
      "s_waitcnt lgkmcnt(6)\n"
      "v_mfma_f32_16x16x32_bf16 v[192:195], v[168:171], v[8:11], v[192:195]\n"
      "v_mul_f32 v196, s98, v196\n"
      "v_mul_f32 v197, s98, v197\n"
      "v_exp_f32 v196, v196\n"
      "s_waitcnt lgkmcnt(5)\n"
      "v_mfma_f32_16x16x32_bf16 v[192:195], v[172:175], v[12:15], v[192:195]\n"
      "v_mul_f32 v198, s98, v198\n"
      "v_mul_f32 v199, s98, v199\n"
      "v_exp_f32 v197, v197\n"
      "s_waitcnt lgkmcnt(4)\n"
      "v_mfma_f32_16x16x32_bf16 v[192:195], v[176:179], v[16:19], v[192:195]\n"
      "v_exp_f32 v198, v198\n"
      "v_add_f32 v224, v224, v196\n"
      "s_waitcnt lgkmcnt(3)\n"
      "v_mfma_f32_16x16x32_bf16 v[192:195], v[180:183], v[20:23], v[192:195]\n"
      "v_exp_f32 v199, v199\n"
      "v_add_f32 v224, v224, v197\n"
      "s_waitcnt lgkmcnt(2)\n"
      "v_mfma_f32_16x16x32_bf16 v[192:195], v[184:187], v[24:27], v[192:195]\n"
      "v_cvt_pk_bf16_f32 v228, v196, v197\n"
      "v_add_f32 v224, v224, v198\n"
      "s_waitcnt lgkmcnt(1)\n"
      "v_mfma_f32_16x16x32_bf16 v[192:195], v[188:191], v[28:31], v[192:195]\n"
      "v_cvt_pk_bf16_f32 v229, v198, v199\n"
      "v_add_f32 v224, v224, v199\n"
      "ds_write_b64 v234, v[228:229] offset:8192\n"
      "s_waitcnt lgkmcnt(0)\n"
      "s_barrier\n"
      "s_add_u32 s54, s54, 1\n"
      "s_cmp_lt_u32 s54, 6\n"
      "s_cbranch_scc1 LQK%=\n"
      "v_mfma_f32_16x16x32_bf16 v[196:199], v[160:163], v[32:35], v[204:207]\n"
      "s_add_u32 m0, s55, 32768\n"
      "s_add_u32 s57, s95, 0\n"
      "buffer_load_dwordx4 v250, s[36:39], s57 offen nt lds\n"
      "v_mfma_f32_16x16x32_bf16 v[196:199], v[164:167], v[36:39], v[196:199]\n"
      "v_mfma_f32_16x16x32_bf16 v[196:199], v[168:171], v[40:43], v[196:199]\n"
      "v_mul_f32 v192, s98, v192\n"
      "v_mul_f32 v193, s98, v193\n"
      "v_exp_f32 v192, v192\n"
      "v_mfma_f32_16x16x32_bf16 v[196:199], v[172:175], v[44:47], v[196:199]\n"
      "s_add_u32 m0, s55, 33792\n"
      "s_add_u32 s57, s95, 128\n"
      "buffer_load_dwordx4 v250, s[36:39], s57 offen nt lds\n"
      "v_mul_f32 v194, s98, v194\n"
      "v_mul_f32 v195, s98, v195\n"
      "v_exp_f32 v193, v193\n"
      "v_mfma_f32_16x16x32_bf16 v[196:199], v[176:179], v[48:51], v[196:199]\n"
      "v_exp_f32 v194, v194\n"
      "v_add_f32 v220, v220, v192\n"
      "v_mfma_f32_16x16x32_bf16 v[196:199], v[180:183], v[52:55], v[196:199]\n"
      "v_exp_f32 v195, v195\n"
      "v_add_f32 v220, v220, v193\n"
      "v_mfma_f32_16x16x32_bf16 v[196:199], v[184:187], v[56:59], v[196:199]\n"
      "s_add_u32 m0, s55, 34816\n"
      "s_add_u32 s57, s95, 256\n"
      "buffer_load_dwordx4 v250, s[36:39], s57 offen nt lds\n"
      "v_cvt_pk_bf16_f32 v228, v192, v193\n"
      "v_add_f32 v220, v220, v194\n"
      "v_mfma_f32_16x16x32_bf16 v[196:199], v[188:191], v[60:63], v[196:199]\n"
      "v_cvt_pk_bf16_f32 v229, v194, v195\n"
      "v_add_f32 v220, v220, v195\n"
      "ds_write_b64 v234, v[228:229] offset:10240\n"
      "v_mfma_f32_16x16x32_bf16 v[192:195], v[160:163], v[64:67], v[208:211]\n"
      "v_mfma_f32_16x16x32_bf16 v[192:195], v[164:167], v[68:71], v[192:195]\n"
      "s_add_u32 m0, s55, 35840\n"
      "s_add_u32 s57, s95, 384\n"
      "buffer_load_dwordx4 v250, s[36:39], s57 offen nt lds\n"
      "v_mfma_f32_16x16x32_bf16 v[192:195], v[168:171], v[72:75], v[192:195]\n"
      "v_mul_f32 v196, s98, v196\n"
      "v_mul_f32 v197, s98, v197\n"
      "v_exp_f32 v196, v196\n"
      "v_mfma_f32_16x16x32_bf16 v[192:195], v[172:175], v[76:79], v[192:195]\n"
      "v_mul_f32 v198, s98, v198\n"
      "v_mul_f32 v199, s98, v199\n"
      "v_exp_f32 v197, v197\n"
      "v_mfma_f32_16x16x32_bf16 v[192:195], v[176:179], v[80:83], v[192:195]\n"
      "s_add_u32 m0, s55, 36864\n"
      "s_add_u32 s57, s95, 4096\n"
      "buffer_load_dwordx4 v250, s[36:39], s57 offen nt lds\n"
      "v_exp_f32 v198, v198\n"
      "v_add_f32 v221, v221, v196\n"
      "v_mfma_f32_16x16x32_bf16 v[192:195], v[180:183], v[84:87], v[192:195]\n"
      "v_exp_f32 v199, v199\n"
      "v_add_f32 v221, v221, v197\n"
      "v_mfma_f32_16x16x32_bf16 v[192:195], v[184:187], v[88:91], v[192:195]\n"
      "v_cvt_pk_bf16_f32 v226, v196, v197\n"
      "v_add_f32 v221, v221, v198\n"
      "v_mfma_f32_16x16x32_bf16 v[192:195], v[188:191], v[92:95], v[192:195]\n"
      "s_add_u32 m0, s55, 37888\n"
      "s_add_u32 s57, s95, 4224\n"
      "buffer_load_dwordx4 v250, s[36:39], s57 offen nt lds\n"
      "v_cvt_pk_bf16_f32 v227, v198, v199\n"
      "v_add_f32 v221, v221, v199\n"
      "ds_write_b64 v234, v[226:227] offset:12288\n"
      "v_mfma_f32_16x16x32_bf16 v[196:199], v[160:163], v[96:99], v[212:215]\n"
      "v_mfma_f32_16x16x32_bf16 v[196:199], v[164:167], v[100:103], v[196:199]\n"
      "v_mfma_f32_16x16x32_bf16 v[196:199], v[168:171], v[104:107], v[196:199]\n"
      "s_add_u32 m0, s55, 38912\n"
      "s_add_u32 s57, s95, 4352\n"
      "buffer_load_dwordx4 v250, s[36:39], s57 offen nt lds\n"
      "v_mul_f32 v192, s98, v192\n"
      "v_mul_f32 v193, s98, v193\n"
      "v_exp_f32 v192, v192\n"
      "v_mfma_f32_16x16x32_bf16 v[196:199], v[172:175], v[108:111], v[196:199]\n"
      "v_mul_f32 v194, s98, v194\n"
      "v_mul_f32 v195, s98, v195\n"
      "v_exp_f32 v193, v193\n"
      "v_mfma_f32_16x16x32_bf16 v[196:199], v[176:179], v[112:115], v[196:199]\n"
      "v_exp_f32 v194, v194\n"
      "v_add_f32 v222, v222, v192\n"
      "v_mfma_f32_16x16x32_bf16 v[196:199], v[180:183], v[116:119], v[196:199]\n"
      "s_add_u32 m0, s55, 39936\n"
      "s_add_u32 s57, s95, 4480\n"
      "buffer_load_dwordx4 v250, s[36:39], s57 offen nt lds\n"
      "s_add_u32 s95, s95, 65536\n"
      "v_exp_f32 v195, v195\n"
      "v_add_f32 v222, v222, v193\n"
      "v_mfma_f32_16x16x32_bf16 v[196:199], v[184:187], v[120:123], v[196:199]\n"
      "v_cvt_pk_bf16_f32 v228, v192, v193\n"
      "v_add_f32 v222, v222, v194\n"
      "v_mfma_f32_16x16x32_bf16 v[196:199], v[188:191], v[124:127], v[196:199]\n"
      "v_cvt_pk_bf16_f32 v229, v194, v195\n"
      "v_add_f32 v222, v222, v195\n"
      "ds_write_b64 v234, v[228:229] offset:14336\n"
      "s_waitcnt vmcnt(8)\n"
      "v_mfma_f32_16x16x32_bf16 v[192:195], v[160:163], v[128:131], v[216:219]\n"
      "ds_read_b128 v[160:163], v232 offset:0\n"
      "v_mfma_f32_16x16x32_bf16 v[192:195], v[164:167], v[132:135], v[192:195]\n"
      "ds_read_b128 v[164:167], v233 offset:0\n"
      "v_mfma_f32_16x16x32_bf16 v[192:195], v[168:171], v[136:139], v[192:195]\n"
      "ds_read_b128 v[168:171], v232 offset:1024\n"
      "v_mul_f32 v196, s98, v196\n"
      "v_mul_f32 v197, s98, v197\n"
      "v_exp_f32 v196, v196\n"
      "v_mfma_f32_16x16x32_bf16 v[192:195], v[172:175], v[140:143], v[192:195]\n"
      "ds_read_b128 v[172:175], v233 offset:1024\n"
      "v_mul_f32 v198, s98, v198\n"
      "v_mul_f32 v199, s98, v199\n"
      "v_exp_f32 v197, v197\n"
      "v_mfma_f32_16x16x32_bf16 v[192:195], v[176:179], v[144:147], v[192:195]\n"
      "ds_read_b128 v[176:179], v232 offset:2048\n"
      "v_exp_f32 v198, v198\n"
      "v_add_f32 v223, v223, v196\n"
      "v_mfma_f32_16x16x32_bf16 v[192:195], v[180:183], v[148:151], v[192:195]\n"
      "ds_read_b128 v[180:183], v233 offset:2048\n"
      "v_exp_f32 v199, v199\n"
      "v_add_f32 v223, v223, v197\n"
      "v_mfma_f32_16x16x32_bf16 v[192:195], v[184:187], v[152:155], v[192:195]\n"
      "ds_read_b128 v[184:187], v232 offset:3072\n"
      "v_cvt_pk_bf16_f32 v226, v196, v197\n"
      "v_add_f32 v223, v223, v198\n"
      "v_mfma_f32_16x16x32_bf16 v[192:195], v[188:191], v[156:159], v[192:195]\n"
      "ds_read_b128 v[188:191], v233 offset:3072\n"
      "v_cvt_pk_bf16_f32 v227, v198, v199\n"
      "v_add_f32 v223, v223, v199\n"
      "ds_write_b64 v234, v[226:227] offset:16384\n"
      "s_waitcnt lgkmcnt(8)\n"
      "v_mfma_f32_16x16x32_bf16 v[196:199], v[160:163], v[0:3], v[200:203]\n"
      "s_waitcnt lgkmcnt(7)\n"
      "v_mfma_f32_16x16x32_bf16 v[196:199], v[164:167], v[4:7], v[196:199]\n"
      "s_waitcnt lgkmcnt(6)\n"
      "v_mfma_f32_16x16x32_bf16 v[196:199], v[168:171], v[8:11], v[196:199]\n"
      "v_mul_f32 v192, s98, v192\n"
      "v_mul_f32 v193, s98, v193\n"
      "v_exp_f32 v192, v192\n"
      "s_waitcnt lgkmcnt(5)\n"
      "v_mfma_f32_16x16x32_bf16 v[196:199], v[172:175], v[12:15], v[196:199]\n"
      "v_mul_f32 v194, s98, v194\n"
      "v_mul_f32 v195, s98, v195\n"
      "v_exp_f32 v193, v193\n"
      "s_waitcnt lgkmcnt(4)\n"
      "v_mfma_f32_16x16x32_bf16 v[196:199], v[176:179], v[16:19], v[196:199]\n"
      "v_exp_f32 v194, v194\n"
      "v_add_f32 v224, v224, v192\n"
      "s_waitcnt lgkmcnt(3)\n"
      "v_mfma_f32_16x16x32_bf16 v[196:199], v[180:183], v[20:23], v[196:199]\n"
      "v_exp_f32 v195, v195\n"
      "v_add_f32 v224, v224, v193\n"
      "s_waitcnt lgkmcnt(2)\n"
      "v_mfma_f32_16x16x32_bf16 v[196:199], v[184:187], v[24:27], v[196:199]\n"
      "v_cvt_pk_bf16_f32 v228, v192, v193\n"
      "v_add_f32 v224, v224, v194\n"
      "s_waitcnt lgkmcnt(1)\n"
      "v_mfma_f32_16x16x32_bf16 v[196:199], v[188:191], v[28:31], v[196:199]\n"
      "v_cvt_pk_bf16_f32 v229, v194, v195\n"
      "v_add_f32 v224, v224, v195\n"
      "ds_write_b64 v234, v[228:229] offset:18432\n"
      "s_waitcnt lgkmcnt(0)\n"
      "s_barrier\n"
      "v_mfma_f32_16x16x32_bf16 v[192:195], v[160:163], v[32:35], v[204:207]\n"
      "v_mfma_f32_16x16x32_bf16 v[192:195], v[164:167], v[36:39], v[192:195]\n"
      "s_nop 1\n"
      "v_mfma_f32_16x16x32_bf16 v[192:195], v[168:171], v[40:43], v[192:195]\n"
      "v_mul_f32 v196, s98, v196\n"
      "v_mul_f32 v197, s98, v197\n"
      "v_exp_f32 v196, v196\n"
      "v_mfma_f32_16x16x32_bf16 v[192:195], v[172:175], v[44:47], v[192:195]\n"
      "v_mul_f32 v198, s98, v198\n"
      "v_mul_f32 v199, s98, v199\n"
      "v_exp_f32 v197, v197\n"
      "v_mfma_f32_16x16x32_bf16 v[192:195], v[176:179], v[48:51], v[192:195]\n"
      "v_exp_f32 v198, v198\n"
      "v_add_f32 v220, v220, v196\n"
      "v_mfma_f32_16x16x32_bf16 v[192:195], v[180:183], v[52:55], v[192:195]\n"
      "v_exp_f32 v199, v199\n"
      "v_add_f32 v220, v220, v197\n"
      "v_mfma_f32_16x16x32_bf16 v[192:195], v[184:187], v[56:59], v[192:195]\n"
      "v_cvt_pk_bf16_f32 v228, v196, v197\n"
      "v_add_f32 v220, v220, v198\n"
      "v_mfma_f32_16x16x32_bf16 v[192:195], v[188:191], v[60:63], v[192:195]\n"
      "v_cvt_pk_bf16_f32 v229, v198, v199\n"
      "v_add_f32 v220, v220, v199\n"
      "ds_write_b64 v234, v[228:229] offset:0\n"
      "v_mfma_f32_16x16x32_bf16 v[196:199], v[160:163], v[64:67], v[208:211]\n"
      "v_mfma_f32_16x16x32_bf16 v[196:199], v[164:167], v[68:71], v[196:199]\n"
      "s_nop 1\n"
      "v_mfma_f32_16x16x32_bf16 v[196:199], v[168:171], v[72:75], v[196:199]\n"
      "v_mul_f32 v192, s98, v192\n"
      "v_mul_f32 v193, s98, v193\n"
      "v_exp_f32 v192, v192\n"
      "v_mfma_f32_16x16x32_bf16 v[196:199], v[172:175], v[76:79], v[196:199]\n"
      "v_mul_f32 v194, s98, v194\n"
      "v_mul_f32 v195, s98, v195\n"
      "v_exp_f32 v193, v193\n"
      "v_mfma_f32_16x16x32_bf16 v[196:199], v[176:179], v[80:83], v[196:199]\n"
      "v_exp_f32 v194, v194\n"
      "v_add_f32 v221, v221, v192\n"
      "v_mfma_f32_16x16x32_bf16 v[196:199], v[180:183], v[84:87], v[196:199]\n"
      "v_exp_f32 v195, v195\n"
      "v_add_f32 v221, v221, v193\n"
      "v_mfma_f32_16x16x32_bf16 v[196:199], v[184:187], v[88:91], v[196:199]\n"
      "v_cvt_pk_bf16_f32 v226, v192, v193\n"
      "v_add_f32 v221, v221, v194\n"
      "v_mfma_f32_16x16x32_bf16 v[196:199], v[188:191], v[92:95], v[196:199]\n"
      "v_cvt_pk_bf16_f32 v227, v194, v195\n"
      "v_add_f32 v221, v221, v195\n"
      "ds_write_b64 v234, v[226:227] offset:2048\n"
      "v_mfma_f32_16x16x32_bf16 v[192:195], v[160:163], v[96:99], v[212:215]\n"
      "v_mfma_f32_16x16x32_bf16 v[192:195], v[164:167], v[100:103], v[192:195]\n"
      "s_nop 1\n"
      "v_mfma_f32_16x16x32_bf16 v[192:195], v[168:171], v[104:107], v[192:195]\n"
      "v_mul_f32 v196, s98, v196\n"
      "v_mul_f32 v197, s98, v197\n"
      "v_exp_f32 v196, v196\n"
      "v_mfma_f32_16x16x32_bf16 v[192:195], v[172:175], v[108:111], v[192:195]\n"
      "v_mul_f32 v198, s98, v198\n"
      "v_mul_f32 v199, s98, v199\n"
      "v_exp_f32 v197, v197\n"
      "v_mfma_f32_16x16x32_bf16 v[192:195], v[176:179], v[112:115], v[192:195]\n"
      "v_exp_f32 v198, v198\n"
      "v_add_f32 v222, v222, v196\n"
      "v_mfma_f32_16x16x32_bf16 v[192:195], v[180:183], v[116:119], v[192:195]\n"
      "v_exp_f32 v199, v199\n"
      "v_add_f32 v222, v222, v197\n"
      "v_mfma_f32_16x16x32_bf16 v[192:195], v[184:187], v[120:123], v[192:195]\n"
      "v_cvt_pk_bf16_f32 v228, v196, v197\n"
      "v_add_f32 v222, v222, v198\n"
      "v_mfma_f32_16x16x32_bf16 v[192:195], v[188:191], v[124:127], v[192:195]\n"
      "v_cvt_pk_bf16_f32 v229, v198, v199\n"
      "v_add_f32 v222, v222, v199\n"
      "ds_write_b64 v234, v[228:229] offset:4096\n"
      "s_waitcnt vmcnt(0)\n"
      "v_mfma_f32_16x16x32_bf16 v[196:199], v[160:163], v[128:131], v[216:219]\n"
      "ds_read_b128 v[160:163], v232 offset:32768\n"
      "v_mfma_f32_16x16x32_bf16 v[196:199], v[164:167], v[132:135], v[196:199]\n"
      "ds_read_b128 v[164:167], v233 offset:32768\n"
      "v_mfma_f32_16x16x32_bf16 v[196:199], v[168:171], v[136:139], v[196:199]\n"
      "ds_read_b128 v[168:171], v232 offset:33792\n"
      "v_mul_f32 v192, s98, v192\n"
      "v_mul_f32 v193, s98, v193\n"
      "v_exp_f32 v192, v192\n"
      "v_mfma_f32_16x16x32_bf16 v[196:199], v[172:175], v[140:143], v[196:199]\n"
      "ds_read_b128 v[172:175], v233 offset:33792\n"
      "v_mul_f32 v194, s98, v194\n"
      "v_mul_f32 v195, s98, v195\n"
      "v_exp_f32 v193, v193\n"
      "v_mfma_f32_16x16x32_bf16 v[196:199], v[176:179], v[144:147], v[196:199]\n"
      "ds_read_b128 v[176:179], v232 offset:34816\n"
      "v_exp_f32 v194, v194\n"
      "v_add_f32 v223, v223, v192\n"
      "v_mfma_f32_16x16x32_bf16 v[196:199], v[180:183], v[148:151], v[196:199]\n"
      "ds_read_b128 v[180:183], v233 offset:34816\n"
      "v_exp_f32 v195, v195\n"
      "v_add_f32 v223, v223, v193\n"
      "v_mfma_f32_16x16x32_bf16 v[196:199], v[184:187], v[152:155], v[196:199]\n"
      "ds_read_b128 v[184:187], v232 offset:35840\n"
      "v_cvt_pk_bf16_f32 v226, v192, v193\n"
      "v_add_f32 v223, v223, v194\n"
      "v_mfma_f32_16x16x32_bf16 v[196:199], v[188:191], v[156:159], v[196:199]\n"
      "ds_read_b128 v[188:191], v233 offset:35840\n"
      "v_cvt_pk_bf16_f32 v227, v194, v195\n"
      "v_add_f32 v223, v223, v195\n"
      "ds_write_b64 v234, v[226:227] offset:6144\n"
      "s_waitcnt lgkmcnt(8)\n"
      "v_mfma_f32_16x16x32_bf16 v[192:195], v[160:163], v[0:3], v[200:203]\n"
      "s_waitcnt lgkmcnt(7)\n"
      "v_mfma_f32_16x16x32_bf16 v[192:195], v[164:167], v[4:7], v[192:195]\n"
      "s_waitcnt lgkmcnt(6)\n"
      "v_mfma_f32_16x16x32_bf16 v[192:195], v[168:171], v[8:11], v[192:195]\n"
      "v_mul_f32 v196, s98, v196\n"
      "v_mul_f32 v197, s98, v197\n"
      "v_exp_f32 v196, v196\n"
      "s_waitcnt lgkmcnt(5)\n"
      "v_mfma_f32_16x16x32_bf16 v[192:195], v[172:175], v[12:15], v[192:195]\n"
      "v_mul_f32 v198, s98, v198\n"
      "v_mul_f32 v199, s98, v199\n"
      "v_exp_f32 v197, v197\n"
      "s_waitcnt lgkmcnt(4)\n"
      "v_mfma_f32_16x16x32_bf16 v[192:195], v[176:179], v[16:19], v[192:195]\n"
      "v_exp_f32 v198, v198\n"
      "v_add_f32 v224, v224, v196\n"
      "s_waitcnt lgkmcnt(3)\n"
      "v_mfma_f32_16x16x32_bf16 v[192:195], v[180:183], v[20:23], v[192:195]\n"
      "v_exp_f32 v199, v199\n"
      "v_add_f32 v224, v224, v197\n"
      "s_waitcnt lgkmcnt(2)\n"
      "v_mfma_f32_16x16x32_bf16 v[192:195], v[184:187], v[24:27], v[192:195]\n"
      "v_cvt_pk_bf16_f32 v228, v196, v197\n"
      "v_add_f32 v224, v224, v198\n"
      "s_waitcnt lgkmcnt(1)\n"
      "v_mfma_f32_16x16x32_bf16 v[192:195], v[188:191], v[28:31], v[192:195]\n"
      "v_cvt_pk_bf16_f32 v229, v198, v199\n"
      "v_add_f32 v224, v224, v199\n"
      "ds_write_b64 v234, v[228:229] offset:8192\n"
      "s_waitcnt lgkmcnt(0)\n"
      "s_barrier\n"
      "v_mfma_f32_16x16x32_bf16 v[196:199], v[160:163], v[32:35], v[204:207]\n"
      "v_mfma_f32_16x16x32_bf16 v[196:199], v[164:167], v[36:39], v[196:199]\n"
      "s_nop 1\n"
      "v_mfma_f32_16x16x32_bf16 v[196:199], v[168:171], v[40:43], v[196:199]\n"
      "v_mul_f32 v192, s98, v192\n"
      "v_mul_f32 v193, s98, v193\n"
      "v_exp_f32 v192, v192\n"
      "v_mfma_f32_16x16x32_bf16 v[196:199], v[172:175], v[44:47], v[196:199]\n"
      "v_mul_f32 v194, s98, v194\n"
      "v_mul_f32 v195, s98, v195\n"
      "v_exp_f32 v193, v193\n"
      "v_mfma_f32_16x16x32_bf16 v[196:199], v[176:179], v[48:51], v[196:199]\n"
      "v_exp_f32 v194, v194\n"
      "v_add_f32 v220, v220, v192\n"
      "v_mfma_f32_16x16x32_bf16 v[196:199], v[180:183], v[52:55], v[196:199]\n"
      "v_exp_f32 v195, v195\n"
      "v_add_f32 v220, v220, v193\n"
      "v_mfma_f32_16x16x32_bf16 v[196:199], v[184:187], v[56:59], v[196:199]\n"
      "v_cvt_pk_bf16_f32 v228, v192, v193\n"
      "v_add_f32 v220, v220, v194\n"
      "v_mfma_f32_16x16x32_bf16 v[196:199], v[188:191], v[60:63], v[196:199]\n"
      "v_cvt_pk_bf16_f32 v229, v194, v195\n"
      "v_add_f32 v220, v220, v195\n"
      "ds_write_b64 v234, v[228:229] offset:10240\n"
      "v_mfma_f32_16x16x32_bf16 v[192:195], v[160:163], v[64:67], v[208:211]\n"
      "v_mfma_f32_16x16x32_bf16 v[192:195], v[164:167], v[68:71], v[192:195]\n"
      "s_nop 1\n"
      "v_mfma_f32_16x16x32_bf16 v[192:195], v[168:171], v[72:75], v[192:195]\n"
      "v_mul_f32 v196, s98, v196\n"
      "v_mul_f32 v197, s98, v197\n"
      "v_exp_f32 v196, v196\n"
      "v_mfma_f32_16x16x32_bf16 v[192:195], v[172:175], v[76:79], v[192:195]\n"
      "v_mul_f32 v198, s98, v198\n"
      "v_mul_f32 v199, s98, v199\n"
      "v_exp_f32 v197, v197\n"
      "v_mfma_f32_16x16x32_bf16 v[192:195], v[176:179], v[80:83], v[192:195]\n"
      "v_exp_f32 v198, v198\n"
      "v_add_f32 v221, v221, v196\n"
      "v_mfma_f32_16x16x32_bf16 v[192:195], v[180:183], v[84:87], v[192:195]\n"
      "v_exp_f32 v199, v199\n"
      "v_add_f32 v221, v221, v197\n"
      "v_mfma_f32_16x16x32_bf16 v[192:195], v[184:187], v[88:91], v[192:195]\n"
      "v_cvt_pk_bf16_f32 v226, v196, v197\n"
      "v_add_f32 v221, v221, v198\n"
      "v_mfma_f32_16x16x32_bf16 v[192:195], v[188:191], v[92:95], v[192:195]\n"
      "v_cvt_pk_bf16_f32 v227, v198, v199\n"
      "v_add_f32 v221, v221, v199\n"
      "ds_write_b64 v234, v[226:227] offset:12288\n"
      "v_mfma_f32_16x16x32_bf16 v[196:199], v[160:163], v[96:99], v[212:215]\n"
      "v_mfma_f32_16x16x32_bf16 v[196:199], v[164:167], v[100:103], v[196:199]\n"
      "s_nop 1\n"
      "v_mfma_f32_16x16x32_bf16 v[196:199], v[168:171], v[104:107], v[196:199]\n"
      "v_mul_f32 v192, s98, v192\n"
      "v_mul_f32 v193, s98, v193\n"
      "v_exp_f32 v192, v192\n"
      "v_mfma_f32_16x16x32_bf16 v[196:199], v[172:175], v[108:111], v[196:199]\n"
      "v_mul_f32 v194, s98, v194\n"
      "v_mul_f32 v195, s98, v195\n"
      "v_exp_f32 v193, v193\n"
      "v_mfma_f32_16x16x32_bf16 v[196:199], v[176:179], v[112:115], v[196:199]\n"
      "v_exp_f32 v194, v194\n"
      "v_add_f32 v222, v222, v192\n"
      "v_mfma_f32_16x16x32_bf16 v[196:199], v[180:183], v[116:119], v[196:199]\n"
      "v_exp_f32 v195, v195\n"
      "v_add_f32 v222, v222, v193\n"
      "v_mfma_f32_16x16x32_bf16 v[196:199], v[184:187], v[120:123], v[196:199]\n"
      "v_cvt_pk_bf16_f32 v228, v192, v193\n"
      "v_add_f32 v222, v222, v194\n"
      "v_mfma_f32_16x16x32_bf16 v[196:199], v[188:191], v[124:127], v[196:199]\n"
      "v_cvt_pk_bf16_f32 v229, v194, v195\n"
      "v_add_f32 v222, v222, v195\n"
      "ds_write_b64 v234, v[228:229] offset:14336\n"
      "v_mfma_f32_16x16x32_bf16 v[192:195], v[160:163], v[128:131], v[216:219]\n"
      "v_mfma_f32_16x16x32_bf16 v[192:195], v[164:167], v[132:135], v[192:195]\n"
      "s_nop 1\n"
      "v_mfma_f32_16x16x32_bf16 v[192:195], v[168:171], v[136:139], v[192:195]\n"
      "v_mul_f32 v196, s98, v196\n"
      "v_mul_f32 v197, s98, v197\n"
      "v_exp_f32 v196, v196\n"
      "v_mfma_f32_16x16x32_bf16 v[192:195], v[172:175], v[140:143], v[192:195]\n"
      "v_mul_f32 v198, s98, v198\n"
      "v_mul_f32 v199, s98, v199\n"
      "v_exp_f32 v197, v197\n"
      "v_mfma_f32_16x16x32_bf16 v[192:195], v[176:179], v[144:147], v[192:195]\n"
      "v_exp_f32 v198, v198\n"
      "v_add_f32 v223, v223, v196\n"
      "v_mfma_f32_16x16x32_bf16 v[192:195], v[180:183], v[148:151], v[192:195]\n"
      "v_exp_f32 v199, v199\n"
      "v_add_f32 v223, v223, v197\n"
      "v_mfma_f32_16x16x32_bf16 v[192:195], v[184:187], v[152:155], v[192:195]\n"
      "v_cvt_pk_bf16_f32 v226, v196, v197\n"
      "v_add_f32 v223, v223, v198\n"
      "v_mfma_f32_16x16x32_bf16 v[192:195], v[188:191], v[156:159], v[192:195]\n"
      "v_cvt_pk_bf16_f32 v227, v198, v199\n"
      "v_add_f32 v223, v223, v199\n"
      "ds_write_b64 v234, v[226:227] offset:16384\n"
      "s_nop 7\n"
      "v_mul_f32 v192, s98, v192\n"
      "v_mul_f32 v193, s98, v193\n"
      "v_exp_f32 v192, v192\n"
      "s_nop 0\n"
      "v_mul_f32 v194, s98, v194\n"
      "v_mul_f32 v195, s98, v195\n"
      "v_exp_f32 v193, v193\n"
      "s_nop 0\n"
      "v_exp_f32 v194, v194\n"
      "v_add_f32 v224, v224, v192\n"
      "s_nop 0\n"
      "v_exp_f32 v195, v195\n"
      "v_add_f32 v224, v224, v193\n"
      "s_nop 0\n"
      "v_cvt_pk_bf16_f32 v228, v192, v193\n"
      "v_add_f32 v224, v224, v194\n"
      "s_nop 0\n"
      "v_cvt_pk_bf16_f32 v229, v194, v195\n"
      "v_add_f32 v224, v224, v195\n"
      "ds_write_b64 v234, v[228:229] offset:18432\n"
      "s_waitcnt lgkmcnt(0)\n"
      "s_barrier\n"
      "v_mov_b32 v192, v220\n"
      "v_mov_b32 v193, v221\n"
      "v_mov_b32 v194, v222\n"
      "v_mov_b32 v195, v223\n"
      "v_mov_b32 v196, v224\n"
      "s_nop 0\n"
      "v_permlane16_swap_b32 v220, v192\n"
      "v_permlane16_swap_b32 v221, v193\n"
      "v_permlane16_swap_b32 v222, v194\n"
      "v_permlane16_swap_b32 v223, v195\n"
      "v_permlane16_swap_b32 v224, v196\n"
      "s_nop 0\n"
      "v_add_f32 v220, v220, v192\n"
      "v_add_f32 v221, v221, v193\n"
      "v_add_f32 v222, v222, v194\n"
      "v_add_f32 v223, v223, v195\n"
      "v_add_f32 v224, v224, v196\n"
      "v_mov_b32 v192, v220\n"
      "v_mov_b32 v193, v221\n"
      "v_mov_b32 v194, v222\n"
      "v_mov_b32 v195, v223\n"
      "v_mov_b32 v196, v224\n"
      "s_nop 0\n"
      "v_permlane32_swap_b32 v220, v192\n"
      "v_permlane32_swap_b32 v221, v193\n"
      "v_permlane32_swap_b32 v222, v194\n"
      "v_permlane32_swap_b32 v223, v195\n"
      "v_permlane32_swap_b32 v224, v196\n"
      "s_nop 0\n"
      "v_add_f32 v220, v220, v192\n"
      "v_add_f32 v221, v221, v193\n"
      "v_add_f32 v222, v222, v194\n"
      "v_add_f32 v223, v223, v195\n"
      "v_add_f32 v224, v224, v196\n"
      "ds_write_b32 v237, v220 offset:1280\n"
      "ds_write_b32 v237, v221 offset:1536\n"
      "ds_write_b32 v237, v222 offset:1792\n"
      "ds_write_b32 v237, v223 offset:2048\n"
      "ds_write_b32 v237, v224 offset:2304\n"
      "s_setprio 0\n"
      "v_bfe_u32 v236, v251, 3, 1\n"
      "v_and_b32 v255, 1, v252\n"
      "v_lshl_or_b32 v236, v255, 1, v236\n"
      "v_bfe_u32 v255, v251, 1, 1\n"
      "v_lshl_or_b32 v236, v236, 1, v255\n"
      "v_lshlrev_b32 v236, 4, v236\n"
      "v_and_b32 v255, 1, v251\n"
      "v_lshl_or_b32 v236, v255, 3, v236\n"
      "v_bfe_u32 v255, v251, 2, 2\n"
      "v_lshl_or_b32 v236, v255, 7, v236\n"
      "v_mul_u32_u24 v255, 7168, v252\n"
      "v_add_u32 v236, v255, v236\n"
      "s_mul_i32 s57, s52, 7\n"
      "s_add_u32 s57, s57, 0\n"
      "s_and_b32 s58, s57, 3\n"
      "s_lshl_b32 s58, s58, 5\n"
      "v_xor_b32 v232, s58, v236\n"
      "s_lshr_b32 s57, s57, 2\n"
      "s_lshl_b32 s57, s57, 10\n"
      "v_add_u32 v232, s57, v232\n"
      "s_mul_i32 s57, s52, 7\n"
      "s_add_u32 s57, s57, 1\n"
      "s_and_b32 s58, s57, 3\n"
      "s_lshl_b32 s58, s58, 5\n"
      "v_xor_b32 v233, s58, v236\n"
      "s_lshr_b32 s57, s57, 2\n"
      "s_lshl_b32 s57, s57, 10\n"
      "v_add_u32 v233, s57, v233\n"
      "s_mul_i32 s57, s52, 7\n"
      "s_add_u32 s57, s57, 2\n"
      "s_and_b32 s58, s57, 3\n"
      "s_lshl_b32 s58, s58, 5\n"
      "v_xor_b32 v234, s58, v236\n"
      "s_lshr_b32 s57, s57, 2\n"
      "s_lshl_b32 s57, s57, 10\n"
      "v_add_u32 v234, s57, v234\n"
      "s_mul_i32 s57, s52, 7\n"
      "s_add_u32 s57, s57, 3\n"
      "s_and_b32 s58, s57, 3\n"
      "s_lshl_b32 s58, s58, 5\n"
      "v_xor_b32 v235, s58, v236\n"
      "s_lshr_b32 s57, s57, 2\n"
      "s_lshl_b32 s57, s57, 10\n"
      "v_add_u32 v235, s57, v235\n"
      "v_add_u32 v236, 57344, v232\n"
      "v_add_u32 v237, 57344, v233\n"
      "v_add_u32 v238, 57344, v234\n"
      "v_add_u32 v239, 57344, v235\n"
      "v_xor_b32 v248, v252, v254\n"
      "v_lshlrev_b32 v248, 4, v248\n"
      "v_lshl_add_u32 v248, v253, 7, v248\n"
      "v_add_u32 v248, 120320, v248\n"
      "v_xor_b32 v249, 64, v248\n"
      "s_waitcnt lgkmcnt(0)\n"
      "s_barrier\n"
      "ds_read_b64_tr_b16 v[160:161], v232 offset:0\n"
      "ds_read_b64_tr_b16 v[162:163], v232 offset:512\n"
      "ds_read_b64_tr_b16 v[164:165], v233 offset:0\n"
      "ds_read_b64_tr_b16 v[166:167], v233 offset:512\n"
      "ds_read_b64_tr_b16 v[168:169], v234 offset:0\n"
      "ds_read_b64_tr_b16 v[170:171], v234 offset:512\n"
      "ds_read_b64_tr_b16 v[172:173], v235 offset:0\n"
      "ds_read_b64_tr_b16 v[174:175], v235 offset:512\n"
      "s_waitcnt lgkmcnt(0)\n"
      "s_barrier\n"
      "ds_read_b128 v[192:195], v248 offset:0\n"
      "ds_read_b128 v[196:199], v248 offset:2048\n"
      "ds_read_b128 v[200:203], v248 offset:4096\n"
      "ds_read_b128 v[204:207], v248 offset:6144\n"
      "ds_read_b128 v[208:211], v248 offset:8192\n"
      "s_waitcnt lgkmcnt(0)\n"
      "v_mfma_f32_16x16x32_bf16 v[0:3], v[160:163], v[192:195], 0\n"
      "ds_read_b64_tr_b16 v[176:177], v232 offset:28672\n"
      "v_mfma_f32_16x16x32_bf16 v[4:7], v[160:163], v[196:199], 0\n"
      "ds_read_b64_tr_b16 v[178:179], v232 offset:29184\n"
      "v_mfma_f32_16x16x32_bf16 v[8:11], v[160:163], v[200:203], 0\n"
      "ds_read_b64_tr_b16 v[180:181], v233 offset:28672\n"
      "v_mfma_f32_16x16x32_bf16 v[12:15], v[160:163], v[204:207], 0\n"
      "ds_read_b64_tr_b16 v[182:183], v233 offset:29184\n"
      "v_mfma_f32_16x16x32_bf16 v[16:19], v[160:163], v[208:211], 0\n"
      "ds_read_b64_tr_b16 v[184:185], v234 offset:28672\n"
      "v_mfma_f32_16x16x32_bf16 v[20:23], v[164:167], v[192:195], 0\n"
      "v_mfma_f32_16x16x32_bf16 v[24:27], v[164:167], v[196:199], 0\n"
      "ds_read_b64_tr_b16 v[186:187], v234 offset:29184\n"
      "v_mfma_f32_16x16x32_bf16 v[28:31], v[164:167], v[200:203], 0\n"
      "ds_read_b64_tr_b16 v[188:189], v235 offset:28672\n"
      "v_mfma_f32_16x16x32_bf16 v[32:35], v[164:167], v[204:207], 0\n"
      "ds_read_b64_tr_b16 v[190:191], v235 offset:29184\n"
      "v_mfma_f32_16x16x32_bf16 v[36:39], v[164:167], v[208:211], 0\n"
      "ds_read_b128 v[212:215], v249 offset:0\n"
      "v_mfma_f32_16x16x32_bf16 v[40:43], v[168:171], v[192:195], 0\n"
      "v_mfma_f32_16x16x32_bf16 v[44:47], v[168:171], v[196:199], 0\n"
      "ds_read_b128 v[216:219], v249 offset:2048\n"
      "v_mfma_f32_16x16x32_bf16 v[48:51], v[168:171], v[200:203], 0\n"
      "ds_read_b128 v[220:223], v249 offset:4096\n"
      "v_mfma_f32_16x16x32_bf16 v[52:55], v[168:171], v[204:207], 0\n"
      "ds_read_b128 v[224:227], v249 offset:6144\n"
      "v_mfma_f32_16x16x32_bf16 v[56:59], v[168:171], v[208:211], 0\n"
      "ds_read_b128 v[228:231], v249 offset:8192\n"
      "v_mfma_f32_16x16x32_bf16 v[60:63], v[172:175], v[192:195], 0\n"
      "v_mfma_f32_16x16x32_bf16 v[64:67], v[172:175], v[196:199], 0\n"
      "v_mfma_f32_16x16x32_bf16 v[68:71], v[172:175], v[200:203], 0\n"
      "v_mfma_f32_16x16x32_bf16 v[72:75], v[172:175], v[204:207], 0\n"
      "v_mfma_f32_16x16x32_bf16 v[76:79], v[172:175], v[208:211], 0\n"
      "s_waitcnt lgkmcnt(0)\n"
      "s_barrier\n"
      "v_mfma_f32_16x16x32_bf16 v[0:3], v[176:179], v[212:215], v[0:3]\n"
      "ds_read_b64_tr_b16 v[160:161], v236 offset:0\n"
      "v_mfma_f32_16x16x32_bf16 v[4:7], v[176:179], v[216:219], v[4:7]\n"
      "ds_read_b64_tr_b16 v[162:163], v236 offset:512\n"
      "v_mfma_f32_16x16x32_bf16 v[8:11], v[176:179], v[220:223], v[8:11]\n"
      "ds_read_b64_tr_b16 v[164:165], v237 offset:0\n"
      "v_mfma_f32_16x16x32_bf16 v[12:15], v[176:179], v[224:227], v[12:15]\n"
      "ds_read_b64_tr_b16 v[166:167], v237 offset:512\n"
      "v_mfma_f32_16x16x32_bf16 v[16:19], v[176:179], v[228:231], v[16:19]\n"
      "ds_read_b64_tr_b16 v[168:169], v238 offset:0\n"
      "v_mfma_f32_16x16x32_bf16 v[20:23], v[180:183], v[212:215], v[20:23]\n"
      "v_mfma_f32_16x16x32_bf16 v[24:27], v[180:183], v[216:219], v[24:27]\n"
      "ds_read_b64_tr_b16 v[170:171], v238 offset:512\n"
      "v_mfma_f32_16x16x32_bf16 v[28:31], v[180:183], v[220:223], v[28:31]\n"
      "ds_read_b64_tr_b16 v[172:173], v239 offset:0\n"
      "v_mfma_f32_16x16x32_bf16 v[32:35], v[180:183], v[224:227], v[32:35]\n"
      "ds_read_b64_tr_b16 v[174:175], v239 offset:512\n"
      "v_mfma_f32_16x16x32_bf16 v[36:39], v[180:183], v[228:231], v[36:39]\n"
      "ds_read_b128 v[192:195], v248 offset:10240\n"
      "v_mfma_f32_16x16x32_bf16 v[40:43], v[184:187], v[212:215], v[40:43]\n"
      "v_mfma_f32_16x16x32_bf16 v[44:47], v[184:187], v[216:219], v[44:47]\n"
      "ds_read_b128 v[196:199], v248 offset:12288\n"
      "v_mfma_f32_16x16x32_bf16 v[48:51], v[184:187], v[220:223], v[48:51]\n"
      "ds_read_b128 v[200:203], v248 offset:14336\n"
      "v_mfma_f32_16x16x32_bf16 v[52:55], v[184:187], v[224:227], v[52:55]\n"
      "ds_read_b128 v[204:207], v248 offset:16384\n"
      "v_mfma_f32_16x16x32_bf16 v[56:59], v[184:187], v[228:231], v[56:59]\n"
      "ds_read_b128 v[208:211], v248 offset:18432\n"
      "v_mfma_f32_16x16x32_bf16 v[60:63], v[188:191], v[212:215], v[60:63]\n"
      "v_mfma_f32_16x16x32_bf16 v[64:67], v[188:191], v[216:219], v[64:67]\n"
      "v_mfma_f32_16x16x32_bf16 v[68:71], v[188:191], v[220:223], v[68:71]\n"
      "v_mfma_f32_16x16x32_bf16 v[72:75], v[188:191], v[224:227], v[72:75]\n"
      "v_mfma_f32_16x16x32_bf16 v[76:79], v[188:191], v[228:231], v[76:79]\n"
      "s_waitcnt lgkmcnt(0)\n"
      "s_barrier\n"
      "v_mfma_f32_16x16x32_bf16 v[0:3], v[160:163], v[192:195], v[0:3]\n"
      "ds_read_b64_tr_b16 v[176:177], v236 offset:28672\n"
      "v_mfma_f32_16x16x32_bf16 v[4:7], v[160:163], v[196:199], v[4:7]\n"
      "ds_read_b64_tr_b16 v[178:179], v236 offset:29184\n"
      "v_mfma_f32_16x16x32_bf16 v[8:11], v[160:163], v[200:203], v[8:11]\n"
      "ds_read_b64_tr_b16 v[180:181], v237 offset:28672\n"
      "v_mfma_f32_16x16x32_bf16 v[12:15], v[160:163], v[204:207], v[12:15]\n"
      "ds_read_b64_tr_b16 v[182:183], v237 offset:29184\n"
      "v_mfma_f32_16x16x32_bf16 v[16:19], v[160:163], v[208:211], v[16:19]\n"
      "ds_read_b64_tr_b16 v[184:185], v238 offset:28672\n"
      "v_mfma_f32_16x16x32_bf16 v[20:23], v[164:167], v[192:195], v[20:23]\n"
      "v_mfma_f32_16x16x32_bf16 v[24:27], v[164:167], v[196:199], v[24:27]\n"
      "ds_read_b64_tr_b16 v[186:187], v238 offset:29184\n"
      "v_mfma_f32_16x16x32_bf16 v[28:31], v[164:167], v[200:203], v[28:31]\n"
      "ds_read_b64_tr_b16 v[188:189], v239 offset:28672\n"
      "v_mfma_f32_16x16x32_bf16 v[32:35], v[164:167], v[204:207], v[32:35]\n"
      "ds_read_b64_tr_b16 v[190:191], v239 offset:29184\n"
      "v_mfma_f32_16x16x32_bf16 v[36:39], v[164:167], v[208:211], v[36:39]\n"
      "ds_read_b128 v[212:215], v249 offset:10240\n"
      "v_mfma_f32_16x16x32_bf16 v[40:43], v[168:171], v[192:195], v[40:43]\n"
      "v_mfma_f32_16x16x32_bf16 v[44:47], v[168:171], v[196:199], v[44:47]\n"
      "ds_read_b128 v[216:219], v249 offset:12288\n"
      "v_mfma_f32_16x16x32_bf16 v[48:51], v[168:171], v[200:203], v[48:51]\n"
      "ds_read_b128 v[220:223], v249 offset:14336\n"
      "v_mfma_f32_16x16x32_bf16 v[52:55], v[168:171], v[204:207], v[52:55]\n"
      "ds_read_b128 v[224:227], v249 offset:16384\n"
      "v_mfma_f32_16x16x32_bf16 v[56:59], v[168:171], v[208:211], v[56:59]\n"
      "ds_read_b128 v[228:231], v249 offset:18432\n"
      "v_mfma_f32_16x16x32_bf16 v[60:63], v[172:175], v[192:195], v[60:63]\n"
      "v_mfma_f32_16x16x32_bf16 v[64:67], v[172:175], v[196:199], v[64:67]\n"
      "v_mfma_f32_16x16x32_bf16 v[68:71], v[172:175], v[200:203], v[68:71]\n"
      "v_mfma_f32_16x16x32_bf16 v[72:75], v[172:175], v[204:207], v[72:75]\n"
      "v_mfma_f32_16x16x32_bf16 v[76:79], v[172:175], v[208:211], v[76:79]\n"
      "s_waitcnt lgkmcnt(0)\n"
      "s_barrier\n"
      "v_mfma_f32_16x16x32_bf16 v[0:3], v[176:179], v[212:215], v[0:3]\n"
      "ds_read_b64_tr_b16 v[160:161], v232 offset:0\n"
      "v_mfma_f32_16x16x32_bf16 v[4:7], v[176:179], v[216:219], v[4:7]\n"
      "ds_read_b64_tr_b16 v[162:163], v232 offset:512\n"
      "v_mfma_f32_16x16x32_bf16 v[8:11], v[176:179], v[220:223], v[8:11]\n"
      "ds_read_b64_tr_b16 v[164:165], v233 offset:0\n"
      "v_mfma_f32_16x16x32_bf16 v[12:15], v[176:179], v[224:227], v[12:15]\n"
      "ds_read_b64_tr_b16 v[166:167], v233 offset:512\n"
      "v_mfma_f32_16x16x32_bf16 v[16:19], v[176:179], v[228:231], v[16:19]\n"
      "ds_read_b64_tr_b16 v[168:169], v234 offset:0\n"
      "v_mfma_f32_16x16x32_bf16 v[20:23], v[180:183], v[212:215], v[20:23]\n"
      "v_mfma_f32_16x16x32_bf16 v[24:27], v[180:183], v[216:219], v[24:27]\n"
      "ds_read_b64_tr_b16 v[170:171], v234 offset:512\n"
      "v_mfma_f32_16x16x32_bf16 v[28:31], v[180:183], v[220:223], v[28:31]\n"
      "ds_read_b64_tr_b16 v[172:173], v235 offset:0\n"
      "v_mfma_f32_16x16x32_bf16 v[32:35], v[180:183], v[224:227], v[32:35]\n"
      "ds_read_b64_tr_b16 v[174:175], v235 offset:512\n"
      "v_mfma_f32_16x16x32_bf16 v[36:39], v[180:183], v[228:231], v[36:39]\n"
      "ds_read_b128 v[192:195], v248 offset:20480\n"
      "v_mfma_f32_16x16x32_bf16 v[40:43], v[184:187], v[212:215], v[40:43]\n"
      "v_mfma_f32_16x16x32_bf16 v[44:47], v[184:187], v[216:219], v[44:47]\n"
      "ds_read_b128 v[196:199], v248 offset:22528\n"
      "v_mfma_f32_16x16x32_bf16 v[48:51], v[184:187], v[220:223], v[48:51]\n"
      "ds_read_b128 v[200:203], v248 offset:24576\n"
      "v_mfma_f32_16x16x32_bf16 v[52:55], v[184:187], v[224:227], v[52:55]\n"
      "ds_read_b128 v[204:207], v248 offset:26624\n"
      "v_mfma_f32_16x16x32_bf16 v[56:59], v[184:187], v[228:231], v[56:59]\n"
      "ds_read_b128 v[208:211], v248 offset:28672\n"
      "v_mfma_f32_16x16x32_bf16 v[60:63], v[188:191], v[212:215], v[60:63]\n"
      "v_mfma_f32_16x16x32_bf16 v[64:67], v[188:191], v[216:219], v[64:67]\n"
      "v_mfma_f32_16x16x32_bf16 v[68:71], v[188:191], v[220:223], v[68:71]\n"
      "v_mfma_f32_16x16x32_bf16 v[72:75], v[188:191], v[224:227], v[72:75]\n"
      "v_mfma_f32_16x16x32_bf16 v[76:79], v[188:191], v[228:231], v[76:79]\n"
      "s_waitcnt lgkmcnt(0)\n"
      "s_barrier\n"
      "v_mfma_f32_16x16x32_bf16 v[0:3], v[160:163], v[192:195], v[0:3]\n"
      "ds_read_b64_tr_b16 v[176:177], v232 offset:28672\n"
      "v_mfma_f32_16x16x32_bf16 v[4:7], v[160:163], v[196:199], v[4:7]\n"
      "ds_read_b64_tr_b16 v[178:179], v232 offset:29184\n"
      "v_mfma_f32_16x16x32_bf16 v[8:11], v[160:163], v[200:203], v[8:11]\n"
      "ds_read_b64_tr_b16 v[180:181], v233 offset:28672\n"
      "v_mfma_f32_16x16x32_bf16 v[12:15], v[160:163], v[204:207], v[12:15]\n"
      "ds_read_b64_tr_b16 v[182:183], v233 offset:29184\n"
      "v_mfma_f32_16x16x32_bf16 v[16:19], v[160:163], v[208:211], v[16:19]\n"
      "ds_read_b64_tr_b16 v[184:185], v234 offset:28672\n"
      "v_mfma_f32_16x16x32_bf16 v[20:23], v[164:167], v[192:195], v[20:23]\n"
      "v_mfma_f32_16x16x32_bf16 v[24:27], v[164:167], v[196:199], v[24:27]\n"
      "ds_read_b64_tr_b16 v[186:187], v234 offset:29184\n"
      "v_mfma_f32_16x16x32_bf16 v[28:31], v[164:167], v[200:203], v[28:31]\n"
      "ds_read_b64_tr_b16 v[188:189], v235 offset:28672\n"
      "v_mfma_f32_16x16x32_bf16 v[32:35], v[164:167], v[204:207], v[32:35]\n"
      "ds_read_b64_tr_b16 v[190:191], v235 offset:29184\n"
      "v_mfma_f32_16x16x32_bf16 v[36:39], v[164:167], v[208:211], v[36:39]\n"
      "ds_read_b128 v[212:215], v249 offset:20480\n"
      "v_mfma_f32_16x16x32_bf16 v[40:43], v[168:171], v[192:195], v[40:43]\n"
      "v_mfma_f32_16x16x32_bf16 v[44:47], v[168:171], v[196:199], v[44:47]\n"
      "ds_read_b128 v[216:219], v249 offset:22528\n"
      "v_mfma_f32_16x16x32_bf16 v[48:51], v[168:171], v[200:203], v[48:51]\n"
      "ds_read_b128 v[220:223], v249 offset:24576\n"
      "v_mfma_f32_16x16x32_bf16 v[52:55], v[168:171], v[204:207], v[52:55]\n"
      "ds_read_b128 v[224:227], v249 offset:26624\n"
      "v_mfma_f32_16x16x32_bf16 v[56:59], v[168:171], v[208:211], v[56:59]\n"
      "ds_read_b128 v[228:231], v249 offset:28672\n"
      "v_mfma_f32_16x16x32_bf16 v[60:63], v[172:175], v[192:195], v[60:63]\n"
      "v_mfma_f32_16x16x32_bf16 v[64:67], v[172:175], v[196:199], v[64:67]\n"
      "v_mfma_f32_16x16x32_bf16 v[68:71], v[172:175], v[200:203], v[68:71]\n"
      "v_mfma_f32_16x16x32_bf16 v[72:75], v[172:175], v[204:207], v[72:75]\n"
      "v_mfma_f32_16x16x32_bf16 v[76:79], v[172:175], v[208:211], v[76:79]\n"
      "s_waitcnt lgkmcnt(0)\n"
      "s_barrier\n"
      "v_mfma_f32_16x16x32_bf16 v[0:3], v[176:179], v[212:215], v[0:3]\n"
      "ds_read_b64_tr_b16 v[160:161], v236 offset:0\n"
      "v_mfma_f32_16x16x32_bf16 v[4:7], v[176:179], v[216:219], v[4:7]\n"
      "ds_read_b64_tr_b16 v[162:163], v236 offset:512\n"
      "v_mfma_f32_16x16x32_bf16 v[8:11], v[176:179], v[220:223], v[8:11]\n"
      "ds_read_b64_tr_b16 v[164:165], v237 offset:0\n"
      "v_mfma_f32_16x16x32_bf16 v[12:15], v[176:179], v[224:227], v[12:15]\n"
      "ds_read_b64_tr_b16 v[166:167], v237 offset:512\n"
      "v_mfma_f32_16x16x32_bf16 v[16:19], v[176:179], v[228:231], v[16:19]\n"
      "ds_read_b64_tr_b16 v[168:169], v238 offset:0\n"
      "v_mfma_f32_16x16x32_bf16 v[20:23], v[180:183], v[212:215], v[20:23]\n"
      "v_mfma_f32_16x16x32_bf16 v[24:27], v[180:183], v[216:219], v[24:27]\n"
      "ds_read_b64_tr_b16 v[170:171], v238 offset:512\n"
      "v_mfma_f32_16x16x32_bf16 v[28:31], v[180:183], v[220:223], v[28:31]\n"
      "ds_read_b64_tr_b16 v[172:173], v239 offset:0\n"
      "v_mfma_f32_16x16x32_bf16 v[32:35], v[180:183], v[224:227], v[32:35]\n"
      "ds_read_b64_tr_b16 v[174:175], v239 offset:512\n"
      "v_mfma_f32_16x16x32_bf16 v[36:39], v[180:183], v[228:231], v[36:39]\n"
      "ds_read_b128 v[192:195], v248 offset:30720\n"
      "v_mfma_f32_16x16x32_bf16 v[40:43], v[184:187], v[212:215], v[40:43]\n"
      "v_mfma_f32_16x16x32_bf16 v[44:47], v[184:187], v[216:219], v[44:47]\n"
      "ds_read_b128 v[196:199], v248 offset:32768\n"
      "v_mfma_f32_16x16x32_bf16 v[48:51], v[184:187], v[220:223], v[48:51]\n"
      "ds_read_b128 v[200:203], v248 offset:34816\n"
      "v_mfma_f32_16x16x32_bf16 v[52:55], v[184:187], v[224:227], v[52:55]\n"
      "ds_read_b128 v[204:207], v248 offset:36864\n"
      "v_mfma_f32_16x16x32_bf16 v[56:59], v[184:187], v[228:231], v[56:59]\n"
      "ds_read_b128 v[208:211], v248 offset:38912\n"
      "v_mfma_f32_16x16x32_bf16 v[60:63], v[188:191], v[212:215], v[60:63]\n"
      "v_mfma_f32_16x16x32_bf16 v[64:67], v[188:191], v[216:219], v[64:67]\n"
      "v_mfma_f32_16x16x32_bf16 v[68:71], v[188:191], v[220:223], v[68:71]\n"
      "v_mfma_f32_16x16x32_bf16 v[72:75], v[188:191], v[224:227], v[72:75]\n"
      "v_mfma_f32_16x16x32_bf16 v[76:79], v[188:191], v[228:231], v[76:79]\n"
      "s_waitcnt lgkmcnt(0)\n"
      "s_barrier\n"
      "v_mfma_f32_16x16x32_bf16 v[0:3], v[160:163], v[192:195], v[0:3]\n"
      "ds_read_b64_tr_b16 v[176:177], v236 offset:28672\n"
      "v_mfma_f32_16x16x32_bf16 v[4:7], v[160:163], v[196:199], v[4:7]\n"
      "ds_read_b64_tr_b16 v[178:179], v236 offset:29184\n"
      "v_mfma_f32_16x16x32_bf16 v[8:11], v[160:163], v[200:203], v[8:11]\n"
      "ds_read_b64_tr_b16 v[180:181], v237 offset:28672\n"
      "v_mfma_f32_16x16x32_bf16 v[12:15], v[160:163], v[204:207], v[12:15]\n"
      "ds_read_b64_tr_b16 v[182:183], v237 offset:29184\n"
      "v_mfma_f32_16x16x32_bf16 v[16:19], v[160:163], v[208:211], v[16:19]\n"
      "ds_read_b64_tr_b16 v[184:185], v238 offset:28672\n"
      "v_mfma_f32_16x16x32_bf16 v[20:23], v[164:167], v[192:195], v[20:23]\n"
      "v_mfma_f32_16x16x32_bf16 v[24:27], v[164:167], v[196:199], v[24:27]\n"
      "ds_read_b64_tr_b16 v[186:187], v238 offset:29184\n"
      "v_mfma_f32_16x16x32_bf16 v[28:31], v[164:167], v[200:203], v[28:31]\n"
      "ds_read_b64_tr_b16 v[188:189], v239 offset:28672\n"
      "v_mfma_f32_16x16x32_bf16 v[32:35], v[164:167], v[204:207], v[32:35]\n"
      "ds_read_b64_tr_b16 v[190:191], v239 offset:29184\n"
      "v_mfma_f32_16x16x32_bf16 v[36:39], v[164:167], v[208:211], v[36:39]\n"
      "ds_read_b128 v[212:215], v249 offset:30720\n"
      "v_mfma_f32_16x16x32_bf16 v[40:43], v[168:171], v[192:195], v[40:43]\n"
      "v_mfma_f32_16x16x32_bf16 v[44:47], v[168:171], v[196:199], v[44:47]\n"
      "ds_read_b128 v[216:219], v249 offset:32768\n"
      "v_mfma_f32_16x16x32_bf16 v[48:51], v[168:171], v[200:203], v[48:51]\n"
      "ds_read_b128 v[220:223], v249 offset:34816\n"
      "v_mfma_f32_16x16x32_bf16 v[52:55], v[168:171], v[204:207], v[52:55]\n"
      "ds_read_b128 v[224:227], v249 offset:36864\n"
      "v_mfma_f32_16x16x32_bf16 v[56:59], v[168:171], v[208:211], v[56:59]\n"
      "ds_read_b128 v[228:231], v249 offset:38912\n"
      "v_mfma_f32_16x16x32_bf16 v[60:63], v[172:175], v[192:195], v[60:63]\n"
      "v_mfma_f32_16x16x32_bf16 v[64:67], v[172:175], v[196:199], v[64:67]\n"
      "v_mfma_f32_16x16x32_bf16 v[68:71], v[172:175], v[200:203], v[68:71]\n"
      "v_mfma_f32_16x16x32_bf16 v[72:75], v[172:175], v[204:207], v[72:75]\n"
      "v_mfma_f32_16x16x32_bf16 v[76:79], v[172:175], v[208:211], v[76:79]\n"
      "s_waitcnt lgkmcnt(0)\n"
      "v_lshlrev_b32 v240, 2, v253\n"
      "global_load_dword %[cs], v240, %[chal] sc1\n"
      "v_mfma_f32_16x16x32_bf16 v[0:3], v[176:179], v[212:215], v[0:3]\n"
      "v_mfma_f32_16x16x32_bf16 v[4:7], v[176:179], v[216:219], v[4:7]\n"
      "v_mfma_f32_16x16x32_bf16 v[8:11], v[176:179], v[220:223], v[8:11]\n"
      "v_mfma_f32_16x16x32_bf16 v[12:15], v[176:179], v[224:227], v[12:15]\n"
      "v_mfma_f32_16x16x32_bf16 v[16:19], v[176:179], v[228:231], v[16:19]\n"
      "v_mfma_f32_16x16x32_bf16 v[20:23], v[180:183], v[212:215], v[20:23]\n"
      "v_mfma_f32_16x16x32_bf16 v[24:27], v[180:183], v[216:219], v[24:27]\n"
      "v_mfma_f32_16x16x32_bf16 v[28:31], v[180:183], v[220:223], v[28:31]\n"
      "v_mfma_f32_16x16x32_bf16 v[32:35], v[180:183], v[224:227], v[32:35]\n"
      "v_mfma_f32_16x16x32_bf16 v[36:39], v[180:183], v[228:231], v[36:39]\n"
      "v_mfma_f32_16x16x32_bf16 v[40:43], v[184:187], v[212:215], v[40:43]\n"
      "v_mfma_f32_16x16x32_bf16 v[44:47], v[184:187], v[216:219], v[44:47]\n"
      "v_mfma_f32_16x16x32_bf16 v[48:51], v[184:187], v[220:223], v[48:51]\n"
      "v_mfma_f32_16x16x32_bf16 v[52:55], v[184:187], v[224:227], v[52:55]\n"
      "v_mfma_f32_16x16x32_bf16 v[56:59], v[184:187], v[228:231], v[56:59]\n"
      "v_mfma_f32_16x16x32_bf16 v[60:63], v[188:191], v[212:215], v[60:63]\n"
      "v_mfma_f32_16x16x32_bf16 v[64:67], v[188:191], v[216:219], v[64:67]\n"
      "v_mfma_f32_16x16x32_bf16 v[68:71], v[188:191], v[220:223], v[68:71]\n"
      "v_mfma_f32_16x16x32_bf16 v[72:75], v[188:191], v[224:227], v[72:75]\n"
      "v_mfma_f32_16x16x32_bf16 v[76:79], v[188:191], v[228:231], v[76:79]\n"
      "s_mov_b64 s[40:41], %[ppart]\n"
      "s_and_b32 s41, s41, 0xffff\n"
      "s_mov_b32 s42, 71680\n"
      "s_mov_b32 s43, 0x00020000\n"
      "v_lshlrev_b32 v248, 4, v251\n"
      "v_lshlrev_b32 v249, 3, v251\n"
      "s_mul_i32 s57, s52, 17920\n"
      "s_nop 4\n"
      "v_cvt_pk_bf16_f32 v160, v0, v1\n"
      "v_cvt_pk_bf16_f32 v161, v2, v3\n"
      "v_cvt_pk_bf16_f32 v162, v20, v21\n"
      "v_cvt_pk_bf16_f32 v163, v22, v23\n"
      "buffer_store_dwordx4 v[160:163], v248, s[40:43], s57 offen offset:0 sc1\n"
      "v_cvt_pk_bf16_f32 v164, v40, v41\n"
      "v_cvt_pk_bf16_f32 v165, v42, v43\n"
      "v_cvt_pk_bf16_f32 v166, v60, v61\n"
      "v_cvt_pk_bf16_f32 v167, v62, v63\n"
      "buffer_store_dwordx4 v[164:167], v248, s[40:43], s57 offen offset:1024 sc1\n"
      "s_add_u32 s57, s57, 3584\n"
      "v_cvt_pk_bf16_f32 v168, v4, v5\n"
      "v_cvt_pk_bf16_f32 v169, v6, v7\n"
      "v_cvt_pk_bf16_f32 v170, v24, v25\n"
      "v_cvt_pk_bf16_f32 v171, v26, v27\n"
      "buffer_store_dwordx4 v[168:171], v248, s[40:43], s57 offen offset:0 sc1\n"
      "v_cvt_pk_bf16_f32 v172, v44, v45\n"
      "v_cvt_pk_bf16_f32 v173, v46, v47\n"
      "v_cvt_pk_bf16_f32 v174, v64, v65\n"
      "v_cvt_pk_bf16_f32 v175, v66, v67\n"
      "buffer_store_dwordx4 v[172:175], v248, s[40:43], s57 offen offset:1024 sc1\n"
      "s_add_u32 s57, s57, 3584\n"
      "v_cvt_pk_bf16_f32 v176, v8, v9\n"
      "v_cvt_pk_bf16_f32 v177, v10, v11\n"
      "v_cvt_pk_bf16_f32 v178, v28, v29\n"
      "v_cvt_pk_bf16_f32 v179, v30, v31\n"
      "buffer_store_dwordx4 v[176:179], v248, s[40:43], s57 offen offset:0 sc1\n"
      "v_cvt_pk_bf16_f32 v180, v48, v49\n"
      "v_cvt_pk_bf16_f32 v181, v50, v51\n"
      "v_cvt_pk_bf16_f32 v182, v68, v69\n"
      "v_cvt_pk_bf16_f32 v183, v70, v71\n"
      "buffer_store_dwordx4 v[180:183], v248, s[40:43], s57 offen offset:1024 sc1\n"
      "s_add_u32 s57, s57, 3584\n"
      "v_cvt_pk_bf16_f32 v184, v12, v13\n"
      "v_cvt_pk_bf16_f32 v185, v14, v15\n"
      "v_cvt_pk_bf16_f32 v186, v32, v33\n"
      "v_cvt_pk_bf16_f32 v187, v34, v35\n"
      "buffer_store_dwordx4 v[184:187], v248, s[40:43], s57 offen offset:0 sc1\n"
      "v_cvt_pk_bf16_f32 v188, v52, v53\n"
      "v_cvt_pk_bf16_f32 v189, v54, v55\n"
      "v_cvt_pk_bf16_f32 v190, v72, v73\n"
      "v_cvt_pk_bf16_f32 v191, v74, v75\n"
      "buffer_store_dwordx4 v[188:191], v248, s[40:43], s57 offen offset:1024 sc1\n"
      "s_add_u32 s57, s57, 3584\n"
      "s_mov_b32 exec_lo, 0x00ff00ff\n"
      "s_mov_b32 exec_hi, 0x00ff00ff\n"
      "v_cvt_pk_bf16_f32 v160, v16, v17\n"
      "v_cvt_pk_bf16_f32 v161, v18, v19\n"
      "v_cvt_pk_bf16_f32 v162, v36, v37\n"
      "v_cvt_pk_bf16_f32 v163, v38, v39\n"
      "buffer_store_dwordx4 v[160:163], v248, s[40:43], s57 offen offset:0 sc1\n"
      "v_cvt_pk_bf16_f32 v164, v56, v57\n"
      "v_cvt_pk_bf16_f32 v165, v58, v59\n"
      "v_cvt_pk_bf16_f32 v166, v76, v77\n"
      "v_cvt_pk_bf16_f32 v167, v78, v79\n"
      "buffer_store_dwordx4 v[164:167], v248, s[40:43], s57 offen offset:1024 sc1\n"
      "s_add_u32 s57, s57, 3584\n"
      "s_mov_b64 exec, -1\n"
      "LEND%=:\n"
      "Lgqa_body_end:\n"
      "s_waitcnt vmcnt(0)\n"
      : [cs] "=&v"(cs)
      : [chal] "s"(chal), [wv] "s"(wv), [jj] "s"(jj), [br] "s"(br), [sub] "s"(sub), [rot] "s"(rot), [pk] "s"(pk), [pv] "s"(pv), [px] "s"(px), [pwq] "s"(pwq),
        [nwq] "s"(nwq), [pwo] "s"(pwo), [ppart] "s"(ppart)
      : "v0", "v1", "v2", "v3", "v4", "v5", "v6", "v7", "v8", "v9", "v10", "v11", "v12", "v13", "v14", "v15", "v16", "v17", "v18", "v19", "v20", "v21", "v22", "v23", "v24", "v25", "v26", "v27", "v28", "v29", "v30", "v31", "v32", "v33", "v34", "v35", "v36", "v37", "v38", "v39", "v40", "v41", "v42", "v43", "v44", "v45", "v46", "v47", "v48", "v49", "v50", "v51", "v52", "v53", "v54", "v55", "v56", "v57", "v58", "v59", "v60", "v61", "v62", "v63", "v64", "v65", "v66", "v67", "v68", "v69", "v70", "v71", "v72", "v73", "v74", "v75", "v76", "v77", "v78", "v79", "v80", "v81", "v82", "v83", "v84", "v85", "v86", "v87", "v88", "v89", "v90", "v91", "v92", "v93", "v94", "v95", "v96", "v97", "v98", "v99", "v100", "v101", "v102", "v103", "v104", "v105", "v106", "v107", "v108", "v109", "v110", "v111", "v112", "v113", "v114", "v115", "v116", "v117", "v118", "v119", "v120", "v121", "v122", "v123", "v124", "v125", "v126", "v127", "v128", "v129", "v130", "v131", "v132", "v133", "v134", "v135", "v136", "v137", "v138", "v139", "v140", "v141", "v142", "v143", "v144", "v145", "v146", "v147", "v148", "v149", "v150", "v151", "v152", "v153", "v154", "v155", "v156", "v157", "v158", "v159", "v160", "v161", "v162", "v163", "v164", "v165", "v166", "v167", "v168", "v169", "v170", "v171", "v172", "v173", "v174", "v175", "v176", "v177", "v178", "v179", "v180", "v181", "v182", "v183", "v184", "v185", "v186", "v187", "v188", "v189", "v190", "v191", "v192", "v193", "v194", "v195", "v196", "v197", "v198", "v199", "v200", "v201", "v202", "v203", "v204", "v205", "v206", "v207", "v208", "v209", "v210", "v211", "v212", "v213", "v214", "v215", "v216", "v217", "v218", "v219", "v220", "v221", "v222", "v223", "v224", "v225", "v226", "v227", "v228", "v229", "v230", "v231", "v232", "v233", "v234", "v235", "v236", "v237", "v238", "v239", "v240", "v241", "v242", "v243", "v244", "v245", "v246", "v247", "v248", "v249", "v250", "v251", "v252", "v253", "v254", "s36", "s37", "s38", "s39", "s40", "s41", "s42", "s43", "s44", "s45", "s46", "s47", "s48", "s49", "s50", "s51", "s52", "s53", "s54", "s55", "s56", "s57", "s58", "s59", "s60", "s61", "s62", "s63", "s64", "s65", "s66", "s67", "s68", "s69", "s70", "s71", "s72", "s73", "s74", "s75", "s76", "s77", "s78", "s79", "s80", "s81", "s82", "s83", "s84", "s85", "s86", "s87", "s88", "s89", "s90", "s91", "s92", "s93", "s94", "s95", "s96", "s97", "s98", "s99", "vcc", "scc", "memory");
  }
  }
  asm volatile("" : "+s"(ka), "+s"(bid), "+s"(ws));
  x = ka->x; kc = ka->kc; vc = ka->vc; wq = ka->wq; wo = ka->wo; out = ka->out;
  const unsigned jj = bid >> 3, b = jj >> 2, br = jj >> 1, h = (bid & 7u) * 2 + (jj & 1u);
  unsigned* chal = (unsigned*)(ws + br * 256u);
  unsigned* echo = (unsigned*)(ws + 65536 + br * 2048u);
  const unsigned lane = lane_id(), g = lane >> 4, c = lane & 15u;
  __builtin_amdgcn_s_barrier();
  if (wv >= 6) return;
  const unsigned cme = __builtin_amdgcn_readlane(cs, h);
  if (wv == 5) {
    bool same = false;
    for (int it = 0; it < 100000; ++it) {
      if (!same && lane < 16) st_sc1(echo + c * 32u + h, cs);
      const bool all = __all(ld_sc1(echo + h * 32u + c) == cme);
      asm volatile("s_waitcnt vmcnt(0)" ::: "memory");
      const unsigned c2 = ld_sc1(chal + c);
      same = __all(c2 == cs);
      cs = c2;
      if (all && same) break;
      if (same) __builtin_amdgcn_s_sleep(4);
    }
    return;
  }
  const unsigned u = h * 5u + wv, qt = u >> 2, j = u & 3u, nq = qt / 5u, T = qt - nq * 5u;
  const unsigned rbase = br * 72u + T * 16u, row = rbase + c;
  const bool live = T * 16u + c < 72u;
  const __amdgpu_buffer_rsrc_t rs = __builtin_amdgcn_make_buffer_rsrc(ws + PART_OFF + (size_t)br * PART_WG + qt * 3584u + j * 1024u, (short)0, 0x7fffffff, 0x00020000);
  const unsigned voff = live ? lane * (j < 3 ? 16u : 8u) : 0x80000000u;
  u32x4 v[HQ];
#pragma unroll
  for (int p = 0; p < HQ; ++p) v[p] = u32x4{0x7fc07fc0u, 0u, 0u, 0u};
  unsigned have = 0;
  for (int it = 0; it < 100000 && have != 0xffffu; ++it) {
    const unsigned ready = (unsigned)__ballot(ld_sc1(echo + h * 32u + c) == cme) & 0xffffu, nw = ready & ~have;
    asm volatile("" ::: "memory");
#pragma unroll
    for (int p = 0; p < HQ; ++p)
      if (nw >> p & 1u) v[p] = __builtin_amdgcn_raw_buffer_load_b128(rs, voff, p * 16u * (unsigned)PART_WG, 16);
    have |= nw;
    if (!nw) __builtin_amdgcn_s_sleep(2);
  }
  if (j == 3) {
#pragma unroll
    for (int p = 0; p < HQ; ++p) { v[p][2] = 0u; v[p][3] = 0u; }
  }
  float s[8] = {0.f, 0.f, 0.f, 0.f, 0.f, 0.f, 0.f, 0.f};
#pragma unroll
  for (int h = 0; h < HQ; ++h)
#pragma unroll
    for (int k = 0; k < 4; ++k) { s[2 * k] += __uint_as_float(v[h][k] << 16); s[2 * k + 1] += __uint_as_float(v[h][k] & 0xffff0000u); }
  const unsigned n0 = nq * 112u + j * 32u + g * 4u;
  bool bad = false;
#pragma unroll
  for (int k = 0; k < 8; ++k) bad |= !(fabsf(s[k]) <= 3.0e38f);
  unsigned long long bm = __ballot(bad && live);
  while (bm) {
    const unsigned cb = (unsigned)__builtin_ctzll(bm) & 15u, rr = rbase + cb;
    bm &= ~(0x0001000100010001ull << cb);
    float* sc = (float*)smem + wv * (SK + 2 * DH); float* qv = sc + SK; float* ov = qv + DH;
    float a8[8] = {0.f, 0.f, 0.f, 0.f, 0.f, 0.f, 0.f, 0.f};
    for (int h = 0; h < HQ; ++h) {
      const unsigned short* K = kc + (size_t)(b * HKV + h / 2) * SK * DH;
      const unsigned short* V = vc + (size_t)(b * HKV + h / 2) * SK * DH;
      for (int d = lane; d < DH; d += 64) {
        float a = 0.f;
        for (int k = 0; k < DM; ++k) a = fmaf(bf2f(x[(size_t)rr * DM + k]), bf2f(wq[(size_t)k * (HQ * DH) + h * DH + d]), a);
        qv[d] = rbf(a);
      }
      __builtin_amdgcn_fence(__ATOMIC_SEQ_CST, "wavefront");
      float m = -__builtin_inff();
      for (int k = lane; k < SK; k += 64) {
        float a = 0.f;
        for (int d = 0; d < DH; ++d) a = fmaf(qv[d], bf2f(K[(size_t)k * DH + d]), a);
        sc[k] = a; m = fmaxf(m, a);
      }
      for (int o = 32; o > 0; o >>= 1) m = fmaxf(m, __shfl_xor(m, o));
      float l = 0.f;
      for (int k = lane; k < SK; k += 64) { const float p = exp2f((sc[k] - m) * CEXP); sc[k] = p; l += p; }
      for (int o = 32; o > 0; o >>= 1) l += __shfl_xor(l, o);
      __builtin_amdgcn_fence(__ATOMIC_SEQ_CST, "wavefront");
      for (int d = lane; d < DH; d += 64) {
        float a = 0.f;
        for (int k = 0; k < SK; ++k) a = fmaf(sc[k], bf2f(V[(size_t)k * DH + d]), a);
        ov[d] = rbf(a / l);
      }
      __builtin_amdgcn_fence(__ATOMIC_SEQ_CST, "wavefront");
      for (int d = 0; d < DH; ++d) {
        const float o = ov[d];
        const unsigned short* wr = wo + (size_t)(h * DH + d) * DM + n0;
#pragma unroll
        for (int k = 0; k < 4; ++k) { a8[k] = fmaf(o, bf2f(wr[k]), a8[k]); if (j < 3) a8[4 + k] = fmaf(o, bf2f(wr[16 + k]), a8[4 + k]); }
      }
      __builtin_amdgcn_fence(__ATOMIC_SEQ_CST, "wavefront");
    }
    if (c == cb) {
#pragma unroll
      for (int k = 0; k < 8; ++k) s[k] = a8[k];
    }
  }
  if (live) {
    unsigned short* po = out + (size_t)row * DM + n0;
    *(u32x2*)po = u32x2{cvtpk(s[0], s[1]), cvtpk(s[2], s[3])};
    if (j < 3) *(u32x2*)(po + 16) = u32x2{cvtpk(s[4], s[5]), cvtpk(s[6], s[7])};
  }
}

extern "C" void kernel_launch(void* const* d_in, const int* in_sizes, int n_in, void* d_out, int out_size, void* d_ws, size_t ws_size,
                              hipStream_t stream) {
  const unsigned short* x = (const unsigned short*)d_in[0];
  const unsigned short* kc = (const unsigned short*)d_in[1];
  const unsigned short* vc = (const unsigned short*)d_in[2];
  const unsigned short* wq = (const unsigned short*)d_in[3];
  const unsigned short* wo = (const unsigned short*)d_in[4];
  (void)hipFuncSetAttribute((const void*)gqa_main, hipFuncAttributeMaxDynamicSharedMemorySize, (int)LDS_BYTES);
  gqa_main<<<256, 512, LDS_BYTES, stream>>>(x, kc, vc, wq, wo, (char*)d_ws, (unsigned short*)d_out);
}
```
